```python
import torch

def _softmax(x, axis=-1):
    return torch.softmax(x, dim=axis)
def _gelu(x):
    return torch.nn.functional.gelu(x, approximate="tanh")
def _silu(x):
    return torch.nn.functional.silu(x)
def _sigmoid(x):
    return torch.sigmoid(x)
def _layernorm(x, axis=-1, eps=1e-5):
    m = x.mean(dim=axis, keepdim=True)
    v = x.var(dim=axis, keepdim=True, unbiased=False)
    return (x - m) / torch.sqrt(v + eps)
def _scatter_add(tgt, idx, src, axis=0):
    return tgt.clone().index_add_(axis, idx, src)
def _segment_sum(src, seg, n):
    out = torch.zeros((n,) + src.shape[1:], dtype=src.dtype)
    return out.index_add_(0, seg, src)
def _pad(x, pw):
    flat = tuple(v for pair in reversed(pw) for v in pair)
    return torch.nn.functional.pad(x, flat)

kernel_name = 'standalone__mha_block_cached__near_ridge'

def setup_inputs():

    _w = (lambda a: (lambda t: t.to(torch.bfloat16) if t.is_floating_point() else t)(torch.as_tensor(a)))
    B, Sq, D = 16, 32, 640
    H, Sk, Dh = 32, 32, 192
    return {
        "x": _w((lambda *s: torch.randn(*s, dtype=torch.bfloat16) * 0.8)(B, Sq, D)),
        "k_cache": _w((lambda *s: torch.randn(*s, dtype=torch.bfloat16) * 0.8)(B, H, Sk, Dh)),
        "v_cache": _w((lambda *s: torch.randn(*s, dtype=torch.bfloat16) * 0.05)(B, H, Sk, Dh)),
        "wq": _w((lambda *s: torch.randn(*s, dtype=torch.bfloat16) * 0.8)(D, H * Dh) * (D ** -0.5)),
        "wo": _w((lambda *s: torch.randn(*s, dtype=torch.bfloat16) * 0.05)(H * Dh, D) * ((H * Dh) ** -0.5)),
    }

def reference(x, k_cache, v_cache, wq, wo):
    B, Sq, D = x.shape
    H, Dh = 32, 192
    q = (lambda x, *d: x.permute(*d))((x @ wq).reshape(B, Sq, H, Dh), 0, 2, 1, 3)
    scores = torch.einsum("bhqd,bhkd->bhqk", q, k_cache) * (Dh ** -0.5)
    p = _softmax(scores, axis=-1)
    o = torch.einsum("bhqk,bhkd->bhqd", p, v_cache)
    return (lambda x, *d: x.permute(*d))(o, 0, 2, 1, 3).reshape(B, Sq, H * Dh) @ wo
```

```cpp
#include <hip/hip_runtime.h>
#include <hip/hip_bf16.h>
typedef __hip_bfloat16 bf16;
constexpr int B=16,SQ=32,D=640,H=32,SK=32,DH=192;
__global__ void qproj(const bf16* x,const bf16* wq,bf16* q){
  int n=blockIdx.x*256+threadIdx.x; int m=blockIdx.y;
  float acc=0;
  for(int k=0;k<D;++k) acc+=__bfloat162float(x[m*D+k])*__bfloat162float(wq[(size_t)k*(H*DH)+n]);
  q[(size_t)m*(H*DH)+n]=__float2bfloat16(acc);
}
__global__ void attn(const bf16* q,const bf16* kc,const bf16* vc,bf16* o){
  int b=blockIdx.x/H,h=blockIdx.x%H; __shared__ float s[32][33];
  int t=threadIdx.x;
  for(int e=t;e<1024;e+=256){int qi=e/32,ki=e%32;float a=0;
    for(int d=0;d<DH;++d)a+=__bfloat162float(q[(size_t)(b*SQ+qi)*(H*DH)+h*DH+d])*__bfloat162float(kc[((size_t)(b*H+h)*SK+ki)*DH+d]);
    s[qi][ki]=a*0.07216878364870323f;}
  __syncthreads();
  if(t<32){float m=-1e30f;for(int k=0;k<32;++k)m=fmaxf(m,s[t][k]);float l=0;for(int k=0;k<32;++k){float e=__expf(s[t][k]-m);s[t][k]=e;l+=e;}
    for(int k=0;k<32;++k)s[t][k]=__bfloat162float(__float2bfloat16(s[t][k]/l));}
  __syncthreads();
  for(int e=t;e<32*DH;e+=256){int qi=e/DH,d=e%DH;float a=0;
    for(int k=0;k<32;++k)a+=s[qi][k]*__bfloat162float(vc[((size_t)(b*H+h)*SK+k)*DH+d]);
    o[(size_t)(b*SQ+qi)*(H*DH)+h*DH+d]=__float2bfloat16(a);}
}
__global__ void oproj(const bf16* o,const bf16* wo,bf16* out){
  int n=blockIdx.x*128+threadIdx.x; int m=blockIdx.y;
  float acc=0;
  for(int k=0;k<H*DH;++k) acc+=__bfloat162float(o[(size_t)m*(H*DH)+k])*__bfloat162float(wo[(size_t)k*D+n]);
  out[m*D+n]=__float2bfloat16(acc);
}
extern "C" void kernel_launch(void* const* d_in, const int* in_sizes, int n_in,
                              void* d_out, int out_size, void* d_ws, size_t ws_size,
                              hipStream_t stream) {
  const bf16*x=(const bf16*)d_in[0],*kc=(const bf16*)d_in[1],*vc=(const bf16*)d_in[2],*wq=(const bf16*)d_in[3],*wo=(const bf16*)d_in[4];
  bf16*q=(bf16*)d_ws; bf16*o=q+512*6144;
  qproj<<<dim3(24,512),256,0,stream>>>(x,wq,q);
  attn<<<512,256,0,stream>>>(q,kc,vc,o);
  oproj<<<dim3(5,512),128,0,stream>>>(o,wo,(bf16*)d_out);
}
```

```cpp
#include <hip/hip_runtime.h>
#include <hip/hip_bf16.h>
typedef __hip_bfloat16 bf16;
constexpr int B=16,SQ=32,D=640,H=32,SK=32,DH=192;
__global__ void qproj(const bf16* x,const bf16* wq,bf16* q){
  int n=blockIdx.x*256+threadIdx.x; int m=blockIdx.y;
  float acc=0;
  for(int k=0;k<D;++k) acc+=__bfloat162float(x[m*D+k])*__bfloat162float(wq[(size_t)k*(H*DH)+n]);
  q[(size_t)m*(H*DH)+n]=__float2bfloat16(acc);
}
__global__ void attn(const bf16* q,const bf16* kc,const bf16* vc,bf16* o){
  int b=blockIdx.x/H,h=blockIdx.x%H; __shared__ float s[32][33];
  int t=threadIdx.x;
  for(int e=t;e<1024;e+=256){int qi=e/32,ki=e%32;float a=0;
    for(int d=0;d<DH;++d)a+=__bfloat162float(q[(size_t)(b*SQ+qi)*(H*DH)+h*DH+d])*__bfloat162float(kc[((size_t)(b*H+h)*SK+ki)*DH+d]);
    s[qi][ki]=a*0.07216878364870323f;}
  __syncthreads();
  if(t<32){float m=-1e30f;for(int k=0;k<32;++k)m=fmaxf(m,s[t][k]);float l=0;for(int k=0;k<32;++k){float e=__expf(s[t][k]-m);s[t][k]=e;l+=e;}
    for(int k=0;k<32;++k)s[t][k]=__bfloat162float(__float2bfloat16(s[t][k]/l));}
  __syncthreads();
  for(int e=t;e<32*DH;e+=256){int qi=e/DH,d=e%DH;float a=0;
    for(int k=0;k<32;++k)a+=s[qi][k]*__bfloat162float(vc[((size_t)(b*H+h)*SK+k)*DH+d]);
    o[(size_t)(b*SQ+qi)*(H*DH)+h*DH+d]=__float2bfloat16(a);}
}
__global__ void oproj(const bf16* o,const bf16* wo,bf16* out){
  int n=blockIdx.x*128+threadIdx.x; int m=blockIdx.y;
  float acc=0;
  for(int k=0;k<H*DH;++k) acc+=__bfloat162float(o[(size_t)m*(H*DH)+k])*__bfloat162float(wo[(size_t)k*D+n]);
  out[m*D+n]=__float2bfloat16(acc);
}
typedef unsigned v4u __attribute__((ext_vector_type(4)));
#define GAS __attribute__((address_space(1)))
#define LAS __attribute__((address_space(3)))
__device__ __forceinline__ unsigned long long rt(){return __builtin_amdgcn_s_memrealtime();}
struct Args{const char*x,*k,*v,*wq,*wo;unsigned long long*st;unsigned*sink;};

template<int NT>
__device__ __forceinline__ void glds(unsigned voff,const char*sbase,unsigned ldsaddr){
  if(NT) asm volatile("s_mov_b32 m0, %2\n\ts_nop 0\n\tglobal_load_lds_dwordx4 %0, %1 nt"::"v"(voff),"s"(sbase),"s"(ldsaddr):"memory");
  else asm volatile("s_mov_b32 m0, %2\n\ts_nop 0\n\tglobal_load_lds_dwordx4 %0, %1"::"v"(voff),"s"(sbase),"s"(ldsaddr):"memory");
}
template<int N> __device__ __forceinline__ void wv(){asm volatile("s_waitcnt vmcnt(%0)"::"i"(N):"memory");}

template<int NW,int DEPTH,int MODE,int NT>
__global__ __launch_bounds__(NW*64,1) void stream(Args a){
  extern __shared__ __attribute__((aligned(16))) char smem[];
  unsigned long long t0=rt();
  int w=__builtin_amdgcn_readfirstlane(threadIdx.x>>6),lane=threadIdx.x&63;
  int bid=blockIdx.x; int i=bid&7, r=bid>>3, bg=r>>2, j=r&3;
  unsigned base=(unsigned)(size_t)smem+w*(DEPTH*1024);
  unsigned long long t1=0,t2=0;
  unsigned vx=(lane>>3)*1280+(lane&7)*16, vq=(lane>>3)*12288+(lane&7)*16, vl=lane*16, vo=(lane>>2)*1280+(lane&3)*16;
  int cnt=0;
  #define ISSUE(VOFF,PTR,N) do{ wv<DEPTH-1>(); glds<N>(VOFF,PTR,base+(cnt%DEPTH)*1024); cnt++; }while(0)
  if(MODE!=3){
    const char*xb=a.x+(size_t)bg*64*1280; const char*qb=a.wq+(4*i+j)*384;
    #pragma unroll
    for(int c=0;c<10;++c){
      #pragma unroll
      for(int u=0;u<8/NW+(NW>8);++u){ISSUE(vx,xb+(size_t)((u*NW+w)*8)*1280+c*128,0);}
      #pragma unroll
      for(int u=0;u<8/NW;++u){
        #pragma unroll
        for(int cb=0;cb<3;++cb){ISSUE(vq,qb+(size_t)(c*64+(u*NW+w)*8)*12288+cb*128,0);}
      }
      if(c==0){t1=rt();}
    }
    #pragma unroll
    for(int u=0;u<24/NW;++u){int p=u*NW+w;int b=p/12,rr=p%12;ISSUE(vl,a.k+((size_t)((2*bg+b)*32+4*i+j))*12288+rr*1024,NT);}
  }
  t2=rt();
  if(MODE!=2){
    #pragma unroll
    for(int hh=0;hh<4;++hh){
      #pragma unroll
      for(int u=0;u<24/NW;++u){int p=u*NW+w;int b=p/12,rr=p%12;ISSUE(vl,a.v+((size_t)((2*bg+b)*32+4*i+hh))*12288+rr*1024,0);}
      if(MODE==0||MODE==3){
        if(NW==4){
        #pragma unroll
        for(int u=0;u<3;++u){
          #pragma unroll
          for(int cb=0;cb<5;++cb){ISSUE(vo,a.wo+(size_t)((4*i+hh)*192+(u*4+w)*16)*1280+j*320+cb*64,0);}
        }} else {
          if((hh&1)==0){
          #pragma unroll
          for(int u=0;u<3;++u){
            #pragma unroll
            for(int cb=0;cb<5;++cb){ISSUE(vo,a.wo+(size_t)((4*i+hh)*192+(u*8+w)*16)*1280+j*320+cb*64,0);}
          }}
        }
      } else {
        #pragma unroll
        for(int u=0;u<24/NW;++u){
          #pragma unroll
          for(int cb=0;cb<3;++cb){ISSUE(vx,a.wo+(size_t)((4*i+hh)*192+(u*NW+w)*8)*1280+((j*320)&~127)+cb*128,0);}
        }
      }
    }
  }
  wv<0>();
  unsigned long long t3=rt();
  if(lane==0){unsigned long long*s=a.st+(bid*16+w)*4;s[0]=t0;s[1]=t1;s[2]=t2;s[3]=t3;}
}

__global__ void amplify(const unsigned long long* st){
  __shared__ unsigned long long smn[256],smx[256];
  int t=threadIdx.x; unsigned long long mn=~0ull,mx=0;
  for(int w=0;w<4;++w){const unsigned long long*s=st+(t*16+w)*4; mn=s[0]<mn?s[0]:mn; mx=s[3]>mx?s[3]:mx;}
  smn[t]=mn;smx[t]=mx;__syncthreads();
  if(t==0){for(int i=0;i<256;++i){mn=smn[i]<mn?smn[i]:mn;mx=smx[i]>mx?smx[i]:mx;}
    unsigned long long d=mx-mn; if(d>10000)d=10000; unsigned long long t0=rt(); while(rt()-t0<d*200)__builtin_amdgcn_s_sleep(8);}
}
extern "C" void kernel_launch(void* const* d_in, const int* in_sizes, int n_in,
                              void* d_out, int out_size, void* d_ws, size_t ws_size,
                              hipStream_t stream_) {
  const bf16*x=(const bf16*)d_in[0],*kc=(const bf16*)d_in[1],*vc=(const bf16*)d_in[2],*wq=(const bf16*)d_in[3],*wo=(const bf16*)d_in[4];
  bf16*q=(bf16*)d_ws; bf16*o=q+512*6144;
  Args a; a.x=(const char*)x;a.k=(const char*)kc;a.v=(const char*)vc;a.wq=(const char*)wq;a.wo=(const char*)wo;
  a.st=(unsigned long long*)((char*)d_ws+(64<<20)); a.sink=(unsigned*)((char*)d_ws+(65<<20));
  hipFuncSetAttribute((void*)stream<4,32,2,0>,hipFuncAttributeMaxDynamicSharedMemorySize,131072);
  stream<4,32,2,0><<<256,256,131072,stream_>>>(a);
  amplify<<<1,256,0,stream_>>>(a.st);
  qproj<<<dim3(24,512),256,0,stream_>>>(x,wq,q);
  attn<<<512,256,0,stream_>>>(q,kc,vc,o);
  oproj<<<dim3(5,512),128,0,stream_>>>(o,wo,(bf16*)d_out);
}
```

```cpp
#include <hip/hip_runtime.h>
#include <hip/hip_bf16.h>

typedef unsigned v4u __attribute__((ext_vector_type(4)));
typedef unsigned v2u __attribute__((ext_vector_type(2)));
typedef short s16x4 __attribute__((ext_vector_type(4)));
typedef short bf16x8 __attribute__((ext_vector_type(8)));
typedef __bf16 bf2 __attribute__((ext_vector_type(2)));
typedef float f32x2 __attribute__((ext_vector_type(2)));
typedef float f32x4 __attribute__((ext_vector_type(4)));
typedef float f32x16 __attribute__((ext_vector_type(16)));
#define GAS __attribute__((address_space(1)))
#define LAS __attribute__((address_space(3)))
#define DI __device__ __forceinline__
typedef GAS unsigned gu32;
#define RLX_AGENT __ATOMIC_RELAXED, __HIP_MEMORY_SCOPE_AGENT
#define SB() __builtin_amdgcn_sched_barrier(0)

#define STAMP 0
#define ROT 1

constexpr int SLOT = 49152, WT = 16384;
constexpr unsigned WS_EP = 0, WS_FS = 4096, WS_FO = WS_FS + 131072, WS_SX = WS_FO + 131072,
                   WS_X = WS_SX + 256 * 8192, WS_END = WS_X + 16 * 16 * 16 * 2560, WS_ST = 32u << 20;
constexpr int kSpinCap = 1 << 18;

DI f32x16 mfma32(v4u a, v4u b, f32x16 c) {
  return __builtin_amdgcn_mfma_f32_32x32x16_bf16(__builtin_bit_cast(bf16x8, a), __builtin_bit_cast(bf16x8, b), c, 0, 0, 0);
}
DI f32x4 mfma16(v4u a, v4u b, f32x4 c) {
  return __builtin_amdgcn_mfma_f32_16x16x32_bf16(__builtin_bit_cast(bf16x8, a), __builtin_bit_cast(bf16x8, b), c, 0, 0, 0);
}
DI unsigned pk(float a, float b) { f32x2 v = {a, b}; return __builtin_bit_cast(unsigned, __builtin_convertvector(v, bf2)); }
DI v4u pack8(const f32x16& x, int s) {
  return (v4u){pk(x[8 * s], x[8 * s + 1]), pk(x[8 * s + 2], x[8 * s + 3]), pk(x[8 * s + 4], x[8 * s + 5]), pk(x[8 * s + 6], x[8 * s + 7])};
}
DI v4u lds128(unsigned a) { return *(const LAS v4u*)a; }
DI v4u ldstr(unsigned a) {
  s16x4 lo = __builtin_amdgcn_ds_read_tr16_b64_v4i16((LAS s16x4*)a);
  s16x4 hi = __builtin_amdgcn_ds_read_tr16_b64_v4i16((LAS s16x4*)(a + 512));
  return __builtin_bit_cast(v4u, __builtin_shufflevector(lo, hi, 0, 1, 2, 3, 4, 5, 6, 7));
}
template <int LOFF, int GOFF> DI void dma(unsigned voff, const char* sbase, unsigned lbase) {
  asm volatile("s_add_u32 m0, %2, %3\n\ts_nop 0\n\tglobal_load_lds_dwordx4 %0, %1 offset:%4"
               :: "v"(voff), "s"(sbase), "s"(lbase), "i"(LOFF - GOFF), "i"(GOFF) : "memory", "scc");
}
template <int N> DI void wait_vm() { asm volatile("s_waitcnt vmcnt(%0)" :: "i"(N) : "memory"); }
DI void bar() { asm volatile("s_waitcnt lgkmcnt(0)\n\ts_barrier" ::: "memory"); }
DI void poll(gu32* p, bool active, unsigned tag) {
  for (int spins = 0; spins < kSpinCap; ++spins) {
    unsigned f = tag;
    if (active) f = __hip_atomic_load(p, RLX_AGENT);
    if (__all(f == tag)) break;
  }
  __builtin_amdgcn_fence(__ATOMIC_ACQUIRE, "wavefront");
}
#define ST(n)

template <int NT, bool QF, bool NEXT>
DI void chunk(f32x16 (&acc)[NT], v4u (&w)[NT], v4u& a, const unsigned (&aa)[4], const unsigned (&wa)[2], unsigned so, unsigned sn) {
#pragma unroll
  for (int s = 0; s < 3; ++s) {
    v4u an = lds128(aa[s + 1] + so);
#pragma unroll
    for (int t = 0; t < NT; ++t) {
      acc[t] = QF ? mfma32(w[t], a, acc[t]) : mfma32(a, w[t], acc[t]);
      w[t] = ldstr(wa[t & 1] + so + (t >> 1) * 8192 + (s + 1) * 2048);
      SB();
    }
    a = an;
  }
#pragma unroll
  for (int t = 0; t < 3; ++t) acc[t] = QF ? mfma32(w[t], a, acc[t]) : mfma32(a, w[t], acc[t]);
  SB();
  if (NEXT) {
    bar();
    v4u an = lds128(aa[0] + sn);
#pragma unroll
    for (int t = 0; t < 3; ++t) w[t] = ldstr(wa[t & 1] + sn + (t >> 1) * 8192);
    SB();
#pragma unroll
    for (int t = 3; t < NT; ++t) {
      acc[t] = QF ? mfma32(w[t], a, acc[t]) : mfma32(a, w[t], acc[t]);
      w[t] = ldstr(wa[t & 1] + sn + (t >> 1) * 8192);
      SB();
    }
    a = an;
  } else {
#pragma unroll
    for (int t = 3; t < NT; ++t) acc[t] = QF ? mfma32(w[t], a, acc[t]) : mfma32(a, w[t], acc[t]);
  }
}
DI unsigned nxt(unsigned so) { return so == 2 * SLOT ? 0u : so + SLOT; }

DI void softmax(f32x16& s, v4u (&p)[2]) {
  const float c = 0.07216878364870323f * 1.4426950408889634f;
  float m = s[0];
#pragma unroll
  for (int r = 1; r < 16; ++r) m = fmaxf(m, s[r]);
  { auto x = __builtin_amdgcn_permlane32_swap(__float_as_uint(m), __float_as_uint(m), false, false);
    m = fmaxf(__uint_as_float(x[0]), __uint_as_float(x[1])); }
  const float mc = -m * c;
  float l = 0.f;
#pragma unroll
  for (int r = 0; r < 16; ++r) { s[r] = __builtin_amdgcn_exp2f(fmaf(s[r], c, mc)); l += s[r]; }
  { auto x = __builtin_amdgcn_permlane32_swap(__float_as_uint(l), __float_as_uint(l), false, false);
    l = __uint_as_float(x[0]) + __uint_as_float(x[1]); }
  const float rl = __builtin_amdgcn_rcpf(l);
#pragma unroll
  for (int r = 0; r < 16; ++r) s[r] *= rl;
  p[0] = pack8(s, 0); p[1] = pack8(s, 1);
}
DI float blo(unsigned x) { return __uint_as_float(x << 16); }
DI float bhi(unsigned x) { return __uint_as_float(x & 0xffff0000u); }

__global__ void __launch_bounds__(512, 2)
mha_block_kernel(const char* __restrict__ x, const char* __restrict__ kc, const char* __restrict__ vc,
                 const char* __restrict__ wq, const char* __restrict__ wo, char* __restrict__ out, char* ws) {
  __shared__ __attribute__((aligned(1024))) char smem[163840];
  const unsigned lb = (unsigned)(size_t)(LAS char*)smem;
  const int wv = __builtin_amdgcn_readfirstlane(threadIdx.x >> 6), lane = threadIdx.x & 63;
  const int bid = blockIdx.x, xi = bid & 7, r_ = bid >> 3, hp = r_ & 1, u = (r_ >> 1) & 3, rg = r_ >> 3;
  const int b = wv & 3;
  const int hbase = 4 * xi + 2 * hp, dh = u >> 1;
  const int cls1 = ROT ? (2 * hp + (u & 1) + rg) & 3 : 0, cls2 = ROT ? (u + rg) & 3 : 0;
  const int hfirst = cls2 >> 1;
  const int member = 2 * xi + hp, ogrp = rg * 4 + u, sgrp = member * 4 + rg;
  ST(0);
  const auto rs = __builtin_amdgcn_make_buffer_rsrc(ws, 0, WS_END, 0x00020000);
  LAS unsigned* tagw = (LAS unsigned*)(lb + 3 * SLOT);
  unsigned tag;

  if (wv < 4) {
    const unsigned l8 = lane >> 3, p = lane & 7, z = lane >> 4;
    const unsigned sw0 = (p ^ z) << 4, sw1 = (p ^ z ^ 4) << 4, sww = (p ^ ((z & 1) << 2)) << 4;
    const unsigned la = lb + b * 4096, lw = lb + WT + b * 2048;
    unsigned va[4], vw[2];
#pragma unroll
    for (int m = 0; m < 4; ++m) va[m] = (8 * m + l8) * 1280 + ((m & 1) ? sw1 : sw0);
#pragma unroll
    for (int m = 0; m < 2; ++m) vw[m] = (8 * m + l8) * 12288 + sww;
    const char* xb = x + (size_t)(rg * 128 + b * 32) * 1280 + dh * 640;
    const char* qb = wq + (size_t)(dh * 320 + b * 16) * 12288 + (hbase + (u & 1)) * 384;
    auto issue = [&](const char* ab, const char* wb, unsigned so) {
      dma<0, 0>(va[0], ab, la + so); dma<1024, 0>(va[1], ab, la + so); dma<2048, 0>(va[2], ab, la + so); dma<3072, 0>(va[3], ab, la + so);
      dma<0, 0>(vw[0], wb, lw + so); dma<8192, 128>(vw[0], wb, lw + so); dma<16384, 256>(vw[0], wb, lw + so);
      dma<1024, 0>(vw[1], wb, lw + so); dma<9216, 128>(vw[1], wb, lw + so); dma<17408, 256>(vw[1], wb, lw + so);
    };
    int c = cls1; unsigned so = 0;
    issue(xb + c * 128, qb + (size_t)c * (64 * 12288), so);
#pragma unroll 1
    for (int n = 1; n < 5; ++n) {
      c = c == 4 ? 0 : c + 1; so = nxt(so);
      issue(xb + c * 128, qb + (size_t)c * (64 * 12288), so);
      wait_vm<10>(); bar(); ST(n);
    }
#pragma unroll
    for (int m = 0; m < 4; ++m) va[m] = (8 * m + l8) * 384 + ((m & 1) ? sw1 : sw0);
#pragma unroll
    for (int m = 0; m < 2; ++m) vw[m] = (8 * m + l8) * 1280 + sww;
    so = nxt(so);
    { const char* kb = kc + (size_t)((rg * 4 + b) * 32 + hbase + (u & 1)) * 12288;
      const char* k0 = kb + dh * 6144; const char* k1 = kb + (dh ^ 1) * 6144;
      const unsigned l0 = la + so + dh * 2048, l1 = la + so + (dh ^ 1) * 2048;
      dma<0, 0>(va[0], k0, l0); dma<16384, 128>(va[0], k0, l0); dma<32768, 256>(va[0], k0, l0);
      dma<1024, 0>(va[1], k0, l0); dma<17408, 128>(va[1], k0, l0); dma<33792, 256>(va[1], k0, l0);
      dma<0, 0>(va[0], k1, l1); dma<16384, 128>(va[0], k1, l1); dma<32768, 256>(va[0], k1, l1);
      dma<1024, 0>(va[1], k1, l1); dma<17408, 128>(va[1], k1, l1); dma<33792, 256>(va[1], k1, l1); }
    wait_vm<12>(); bar(); ST(5);
    const char* vb = vc + (size_t)((rg * 4 + b) * 32 + hbase) * 12288;
    const char* ob = wo + (size_t)(hbase * 192 + b * 16) * 1280 + ((u * 320) & ~127);
#pragma unroll 1
    for (int n = 0; n < 6; ++n) {
      const int hh = (n >= 3) ^ hfirst, t0 = n >= 3 ? n - 3 : n, t = (cls2 & 1) ? 2 - t0 : t0;
      so = nxt(so);
      issue(vb + hh * 12288 + t * 128, ob + (size_t)(hh * 192 + t * 64) * 1280, so);
      wait_vm<10>(); bar(); ST(6 + n);
    }
    wait_vm<0>(); bar(); ST(12);
    tag = __builtin_amdgcn_readfirstlane(*tagw);
  } else {
    tag = __builtin_amdgcn_readfirstlane(__hip_atomic_load((gu32*)(ws + WS_EP + rg * 256), RLX_AGENT)) + 1u;
    if (wv == 4 && lane == 0) *tagw = tag;
    const unsigned r = lane & 31, h2 = lane >> 5;
    unsigned aa[4], wa[2];
#pragma unroll
    for (int s = 0; s < 4; ++s) aa[s] = lb + (b * 32 + r) * 128 + (((2 * s + h2) ^ ((r >> 1) & 7)) << 4);
    { const unsigned blk = (lane >> 4) & 1, q = (lane & 15) >> 2, p = lane & 3;
#pragma unroll
      for (int cb = 0; cb < 2; ++cb) wa[cb] = lb + WT + (8 * h2 + q) * 128 + ((4 * (cb ^ (q >> 1)) + 2 * blk + (p & 1)) << 4) + 8 * (p >> 1); }
    unsigned so = 0;
    f32x16 sacc = {};
    {
      f32x16 acc[6] = {};
      v4u w[6], a;
      bar(); ST(1);
      a = lds128(aa[0]);
#pragma unroll
      for (int t = 0; t < 6; ++t) w[t] = ldstr(wa[t & 1] + (t >> 1) * 8192);
      SB();
#pragma unroll 1
      for (int n = 0; n < 4; ++n) { const unsigned sn = nxt(so); chunk<6, true, true>(acc, w, a, aa, wa, so, sn); so = sn; }
      chunk<6, true, false>(acc, w, a, aa, wa, so, 0);
      ST(2);
      v4u qf[12];
#pragma unroll
      for (int t = 0; t < 6; ++t) { qf[2 * t] = pack8(acc[t], 0); qf[2 * t + 1] = pack8(acc[t], 1); }
      so = nxt(so);
      bar(); ST(3);
#pragma unroll
      for (int k = 0; k < 12; ++k) sacc = mfma32(lds128(aa[k & 3] + so + (k >> 2) * 16384), qf[k], sacc);
    }
    { const unsigned o = WS_SX + ((sgrp * 4 + u) * 4 + b) * 2048;
      __builtin_amdgcn_raw_buffer_store_b128(pack8(sacc, 0), rs, lane * 16, o, 16);
      __builtin_amdgcn_raw_buffer_store_b128(pack8(sacc, 1), rs, lane * 16 + 1024, o, 16); }
    ST(4);
    if (u & 1) { const unsigned t = wa[0]; wa[0] = wa[1]; wa[1] = t + 8192; }
    f32x16 oacc[5] = {};
    v4u pf[2][2];
    {
      v4u w[5], a;
      so = nxt(so);
      bar(); ST(5);
      a = lds128(aa[0] + so);
#pragma unroll
      for (int t = 0; t < 5; ++t) w[t] = ldstr(wa[t & 1] + so + (t >> 1) * 8192);
      wait_vm<0>();
      if (lane == 0) __hip_atomic_store((gu32*)(ws + WS_FS + ((sgrp * 4 + u) * 4 + b) * 128), tag, RLX_AGENT);
      SB();
      {
        f32x16 acc[5] = {};
#pragma unroll 1
        for (int n = 0; n < 3; ++n) { const unsigned sn = nxt(so); chunk<5, false, true>(acc, w, a, aa, wa, so, sn); so = sn; }
        ST(6);
        poll((gu32*)(ws + WS_FS + ((sgrp * 4 + (lane & 3)) * 4 + b) * 128), lane < 4, tag);
        ST(7);
        v4u sp[2][2][2];
#pragma unroll
        for (int e = 0; e < 2; ++e)
#pragma unroll
          for (int d = 0; d < 2; ++d)
#pragma unroll
            for (int s = 0; s < 2; ++s)
              sp[e][d][s] = __builtin_amdgcn_raw_buffer_load_b128(rs, lane * 16 + s * 1024, WS_SX + ((sgrp * 4 + 2 * d + (e ^ hfirst)) * 4 + b) * 2048, 16);
#pragma unroll
        for (int e = 0; e < 2; ++e) {
          f32x16 sv;
#pragma unroll
          for (int s = 0; s < 2; ++s)
#pragma unroll
            for (int j = 0; j < 4; ++j) {
              sv[8 * s + 2 * j] = blo(sp[e][0][s][j]) + blo(sp[e][1][s][j]);
              sv[8 * s + 2 * j + 1] = bhi(sp[e][0][s][j]) + bhi(sp[e][1][s][j]);
            }
          softmax(sv, pf[e]);
        }
        ST(8);
#pragma unroll
        for (int t = 0; t < 5; ++t) { oacc[t] = mfma32(pack8(acc[t], 0), pf[0][0], oacc[t]); oacc[t] = mfma32(pack8(acc[t], 1), pf[0][1], oacc[t]); }
      }
      SB();
      {
        f32x16 acc[5] = {};
#pragma unroll 1
        for (int n = 0; n < 2; ++n) { const unsigned sn = nxt(so); chunk<5, false, true>(acc, w, a, aa, wa, so, sn); so = sn; }
        chunk<5, false, false>(acc, w, a, aa, wa, so, 0);
        ST(9);
#pragma unroll
        for (int t = 0; t < 5; ++t) { oacc[t] = mfma32(pack8(acc[t], 0), pf[1][0], oacc[t]); oacc[t] = mfma32(pack8(acc[t], 1), pf[1][1], oacc[t]); }
      }
    }
    { const unsigned vo = (r >> 3) * (16 * 2560) + h2 * 128 + (r & 7) * 16;
      const unsigned o = WS_X + ((ogrp * 16 + 4 * b) * 16 + member) * 2560;
#pragma unroll
      for (int t = 0; t < 5; ++t) {
        __builtin_amdgcn_raw_buffer_store_b128(pack8(oacc[t], 0), rs, vo + t * 512, o, 16);
        __builtin_amdgcn_raw_buffer_store_b128(pack8(oacc[t], 1), rs, vo + t * 512 + 256, o, 16);
      } }
    ST(10);
    wait_vm<0>();
    if (lane == 0) __hip_atomic_store((gu32*)(ws + WS_FO + ((ogrp * 16 + member) * 4 + b) * 128), tag, RLX_AGENT);
    ST(11);
  }
  if (wv < 5) {
    poll((gu32*)(ws + WS_FO + ((ogrp * 16 + (lane & 15)) * 4 + (member >> 2)) * 128), lane < 16, tag);
    ST(13);
    const unsigned g = lane >> 4, c = lane & 15;
    const unsigned vo = (g >> 1) * 2560 + (g & 1) * 256 + c * 16;
    const unsigned o = WS_X + (ogrp * 16 + member) * (16 * 2560) + wv * 512;
    v4u d[8];
#pragma unroll
    for (int e = 0; e < 8; ++e) d[e] = __builtin_amdgcn_raw_buffer_load_b128(rs, vo, o + e * 5120, 16);
    v4u id;
    { const bool on = (c >> 3) == (g & 1); const unsigned one = (c & 1) ? 0x3f800000u : 0x00003f80u, j2 = (c & 7) >> 1;
      id = (v4u){on && j2 == 0 ? one : 0u, on && j2 == 1 ? one : 0u, on && j2 == 2 ? one : 0u, on && j2 == 3 ? one : 0u}; }
    f32x4 sum = {};
#pragma unroll
    for (int e = 0; e < 8; ++e) sum = mfma16(id, d[e], sum);
    const unsigned row = rg * 128 + member * 8 + (c & 7), col = u * 160 + wv * 32 + (g >> 1) * 16 + (c >> 3) * 8 + (g & 1) * 4;
    *(GAS v2u*)(out + row * 1280 + col * 2) = (v2u){pk(sum[0], sum[1]), pk(sum[2], sum[3])};
    if (wv == 0 && member == 0 && u == 0 && lane == 0) __hip_atomic_store((gu32*)(ws + WS_EP + rg * 256), tag, RLX_AGENT);
    ST(14);
  }
}

extern "C" void kernel_launch(void* const* d_in, const int* in_sizes, int n_in, void* d_out, int out_size,
                              void* d_ws, size_t ws_size, hipStream_t stream) {
  mha_block_kernel<<<256, 512, 0, stream>>>((const char*)d_in[0], (const char*)d_in[1], (const char*)d_in[2],
                                            (const char*)d_in[3], (const char*)d_in[4], (char*)d_out, (char*)d_ws);
}
```

```cpp
#define STAMP 1
#include <hip/hip_runtime.h>
#include <hip/hip_bf16.h>

typedef unsigned v4u __attribute__((ext_vector_type(4)));
typedef unsigned v2u __attribute__((ext_vector_type(2)));
typedef short s16x4 __attribute__((ext_vector_type(4)));
typedef short bf16x8 __attribute__((ext_vector_type(8)));
typedef __bf16 bf2 __attribute__((ext_vector_type(2)));
typedef float f32x2 __attribute__((ext_vector_type(2)));
typedef float f32x4 __attribute__((ext_vector_type(4)));
typedef float f32x16 __attribute__((ext_vector_type(16)));
#define GAS __attribute__((address_space(1)))
#define LAS __attribute__((address_space(3)))
#define DI __device__ __forceinline__
typedef GAS unsigned gu32;
#define RLX_AGENT __ATOMIC_RELAXED, __HIP_MEMORY_SCOPE_AGENT
#define SB() __builtin_amdgcn_sched_barrier(0)

#define ROT 1

constexpr int SLOT = 49152, WT = 16384;
constexpr unsigned WS_EP = 0, WS_FS = 4096, WS_FO = WS_FS + 131072, WS_SX = WS_FO + 131072,
                   WS_X = WS_SX + 256 * 8192, WS_END = WS_X + 16 * 16 * 16 * 2560, WS_ST = 32u << 20;
constexpr int kSpinCap = 1 << 18;

DI f32x16 mfma32(v4u a, v4u b, f32x16 c) {
  return __builtin_amdgcn_mfma_f32_32x32x16_bf16(__builtin_bit_cast(bf16x8, a), __builtin_bit_cast(bf16x8, b), c, 0, 0, 0);
}
DI f32x4 mfma16(v4u a, v4u b, f32x4 c) {
  return __builtin_amdgcn_mfma_f32_16x16x32_bf16(__builtin_bit_cast(bf16x8, a), __builtin_bit_cast(bf16x8, b), c, 0, 0, 0);
}
DI unsigned pk(float a, float b) { f32x2 v = {a, b}; return __builtin_bit_cast(unsigned, __builtin_convertvector(v, bf2)); }
DI v4u pack8(const f32x16& x, int s) {
  return (v4u){pk(x[8 * s], x[8 * s + 1]), pk(x[8 * s + 2], x[8 * s + 3]), pk(x[8 * s + 4], x[8 * s + 5]), pk(x[8 * s + 6], x[8 * s + 7])};
}
DI v4u lds128(unsigned a) { return *(const LAS v4u*)a; }
DI v4u ldstr(unsigned a) {
  s16x4 lo = __builtin_amdgcn_ds_read_tr16_b64_v4i16((LAS s16x4*)a);
  s16x4 hi = __builtin_amdgcn_ds_read_tr16_b64_v4i16((LAS s16x4*)(a + 512));
  return __builtin_bit_cast(v4u, __builtin_shufflevector(lo, hi, 0, 1, 2, 3, 4, 5, 6, 7));
}
template <int LOFF, int GOFF> DI void dma(unsigned voff, const char* sbase, unsigned lbase) {
  asm volatile("s_add_u32 m0, %2, %3\n\ts_nop 0\n\tglobal_load_lds_dwordx4 %0, %1 offset:%4"
               :: "v"(voff), "s"(sbase), "s"(lbase), "i"(LOFF - GOFF), "i"(GOFF) : "memory", "scc");
}
template <int N> DI void wait_vm() { asm volatile("s_waitcnt vmcnt(%0)" :: "i"(N) : "memory"); }
DI void bar() { asm volatile("s_waitcnt lgkmcnt(0)\n\ts_barrier" ::: "memory"); }
DI void poll(gu32* p, bool active, unsigned tag) {
  for (int spins = 0; spins < kSpinCap; ++spins) {
    unsigned f = tag;
    if (active) f = __hip_atomic_load(p, RLX_AGENT);
    if (__all(f == tag)) break;
  }
  __builtin_amdgcn_fence(__ATOMIC_ACQUIRE, "wavefront");
}
#define ST(n) do { unsigned long long t_ = __builtin_amdgcn_s_memrealtime(); if (lane == 0) ((LAS unsigned long long*)(lb + 3 * SLOT + 1024))[wv * 32 + (n)] = t_; } while (0)
#define ST_INIT() do { if (lane < 32) ((LAS unsigned long long*)(lb + 3 * SLOT + 1024))[wv * 32 + lane] = 0; } while (0)
#define ST_DUMP() do { wait_vm<0>(); ST(31); if (lane < 32) ((GAS unsigned long long*)(ws + WS_ST))[(bid * 8 + wv) * 32 + lane] = ((LAS unsigned long long*)(lb + 3 * SLOT + 1024))[wv * 32 + lane]; } while (0)

template <int NT, bool QF, bool NEXT>
DI void chunk(f32x16 (&acc)[NT], v4u (&w)[NT], v4u& a, const unsigned (&aa)[4], const unsigned (&wa)[2], unsigned so, unsigned sn) {
#pragma unroll
  for (int s = 0; s < 3; ++s) {
    v4u an = lds128(aa[s + 1] + so);
#pragma unroll
    for (int t = 0; t < NT; ++t) {
      acc[t] = QF ? mfma32(w[t], a, acc[t]) : mfma32(a, w[t], acc[t]);
      w[t] = ldstr(wa[t & 1] + so + (t >> 1) * 8192 + (s + 1) * 2048);
      SB();
    }
    a = an;
  }
#pragma unroll
  for (int t = 0; t < 3; ++t) acc[t] = QF ? mfma32(w[t], a, acc[t]) : mfma32(a, w[t], acc[t]);
  SB();
  if (NEXT) {
    bar();
    v4u an = lds128(aa[0] + sn);
#pragma unroll
    for (int t = 0; t < 3; ++t) w[t] = ldstr(wa[t & 1] + sn + (t >> 1) * 8192);
    SB();
#pragma unroll
    for (int t = 3; t < NT; ++t) {
      acc[t] = QF ? mfma32(w[t], a, acc[t]) : mfma32(a, w[t], acc[t]);
      w[t] = ldstr(wa[t & 1] + sn + (t >> 1) * 8192);
      SB();
    }
    a = an;
  } else {
#pragma unroll
    for (int t = 3; t < NT; ++t) acc[t] = QF ? mfma32(w[t], a, acc[t]) : mfma32(a, w[t], acc[t]);
  }
}
DI unsigned nxt(unsigned so) { return so == 2 * SLOT ? 0u : so + SLOT; }

DI void softmax(f32x16& s, v4u (&p)[2]) {
  const float c = 0.07216878364870323f * 1.4426950408889634f;
  float m = s[0];
#pragma unroll
  for (int r = 1; r < 16; ++r) m = fmaxf(m, s[r]);
  { auto x = __builtin_amdgcn_permlane32_swap(__float_as_uint(m), __float_as_uint(m), false, false);
    m = fmaxf(__uint_as_float(x[0]), __uint_as_float(x[1])); }
  const float mc = -m * c;
  float l = 0.f;
#pragma unroll
  for (int r = 0; r < 16; ++r) { s[r] = __builtin_amdgcn_exp2f(fmaf(s[r], c, mc)); l += s[r]; }
  { auto x = __builtin_amdgcn_permlane32_swap(__float_as_uint(l), __float_as_uint(l), false, false);
    l = __uint_as_float(x[0]) + __uint_as_float(x[1]); }
  const float rl = __builtin_amdgcn_rcpf(l);
#pragma unroll
  for (int r = 0; r < 16; ++r) s[r] *= rl;
  p[0] = pack8(s, 0); p[1] = pack8(s, 1);
}
DI float blo(unsigned x) { return __uint_as_float(x << 16); }
DI float bhi(unsigned x) { return __uint_as_float(x & 0xffff0000u); }

__global__ void __launch_bounds__(512, 2)
mha_block_kernel(const char* __restrict__ x, const char* __restrict__ kc, const char* __restrict__ vc,
                 const char* __restrict__ wq, const char* __restrict__ wo, char* __restrict__ out, char* ws) {
  __shared__ __attribute__((aligned(1024))) char smem[163840];
  const unsigned lb = (unsigned)(size_t)(LAS char*)smem;
  const int wv = __builtin_amdgcn_readfirstlane(threadIdx.x >> 6), lane = threadIdx.x & 63;
  const int bid = blockIdx.x, xi = bid & 7, r_ = bid >> 3, hp = r_ & 1, u = (r_ >> 1) & 3, rg = r_ >> 3;
  const int b = wv & 3;
  const int hbase = 4 * xi + 2 * hp, dh = u >> 1;
  const int cls1 = ROT ? (2 * hp + (u & 1) + rg) & 3 : 0, cls2 = ROT ? (u + rg) & 3 : 0;
  const int hfirst = cls2 >> 1;
  const int member = 2 * xi + hp, ogrp = rg * 4 + u, sgrp = member * 4 + rg;
  ST_INIT(); ST(0);
  const auto rs = __builtin_amdgcn_make_buffer_rsrc(ws, 0, WS_END, 0x00020000);
  LAS unsigned* tagw = (LAS unsigned*)(lb + 3 * SLOT);
  unsigned tag;

  if (wv < 4) {
    const unsigned l8 = lane >> 3, p = lane & 7, z = lane >> 4;
    const unsigned sw0 = (p ^ z) << 4, sw1 = (p ^ z ^ 4) << 4, sww = (p ^ ((z & 1) << 2)) << 4;
    const unsigned la = lb + b * 4096, lw = lb + WT + b * 2048;
    unsigned va[4], vw[2];
#pragma unroll
    for (int m = 0; m < 4; ++m) va[m] = (8 * m + l8) * 1280 + ((m & 1) ? sw1 : sw0);
#pragma unroll
    for (int m = 0; m < 2; ++m) vw[m] = (8 * m + l8) * 12288 + sww;
    const char* xb = x + (size_t)(rg * 128 + b * 32) * 1280 + dh * 640;
    const char* qb = wq + (size_t)(dh * 320 + b * 16) * 12288 + (hbase + (u & 1)) * 384;
    auto issue = [&](const char* ab, const char* wb, unsigned so) {
      dma<0, 0>(va[0], ab, la + so); dma<1024, 0>(va[1], ab, la + so); dma<2048, 0>(va[2], ab, la + so); dma<3072, 0>(va[3], ab, la + so);
      dma<0, 0>(vw[0], wb, lw + so); dma<8192, 128>(vw[0], wb, lw + so); dma<16384, 256>(vw[0], wb, lw + so);
      dma<1024, 0>(vw[1], wb, lw + so); dma<9216, 128>(vw[1], wb, lw + so); dma<17408, 256>(vw[1], wb, lw + so);
    };
    int c = cls1; unsigned so = 0;
    issue(xb + c * 128, qb + (size_t)c * (64 * 12288), so);
#pragma unroll 1
    for (int n = 1; n < 5; ++n) {
      c = c == 4 ? 0 : c + 1; so = nxt(so);
      issue(xb + c * 128, qb + (size_t)c * (64 * 12288), so);
      wait_vm<10>(); bar(); ST(n);
    }
#pragma unroll
    for (int m = 0; m < 4; ++m) va[m] = (8 * m + l8) * 384 + ((m & 1) ? sw1 : sw0);
#pragma unroll
    for (int m = 0; m < 2; ++m) vw[m] = (8 * m + l8) * 1280 + sww;
    so = nxt(so);
    { const char* kb = kc + (size_t)((rg * 4 + b) * 32 + hbase + (u & 1)) * 12288;
      const char* k0 = kb + dh * 6144; const char* k1 = kb + (dh ^ 1) * 6144;
      const unsigned l0 = la + so + dh * 2048, l1 = la + so + (dh ^ 1) * 2048;
      dma<0, 0>(va[0], k0, l0); dma<16384, 128>(va[0], k0, l0); dma<32768, 256>(va[0], k0, l0);
      dma<1024, 0>(va[1], k0, l0); dma<17408, 128>(va[1], k0, l0); dma<33792, 256>(va[1], k0, l0);
      dma<0, 0>(va[0], k1, l1); dma<16384, 128>(va[0], k1, l1); dma<32768, 256>(va[0], k1, l1);
      dma<1024, 0>(va[1], k1, l1); dma<17408, 128>(va[1], k1, l1); dma<33792, 256>(va[1], k1, l1); }
    wait_vm<12>(); bar(); ST(5);
    const char* vb = vc + (size_t)((rg * 4 + b) * 32 + hbase) * 12288;
    const char* ob = wo + (size_t)(hbase * 192 + b * 16) * 1280 + ((u * 320) & ~127);
#pragma unroll 1
    for (int n = 0; n < 6; ++n) {
      const int hh = (n >= 3) ^ hfirst, t0 = n >= 3 ? n - 3 : n, t = (cls2 & 1) ? 2 - t0 : t0;
      so = nxt(so);
      issue(vb + hh * 12288 + t * 128, ob + (size_t)(hh * 192 + t * 64) * 1280, so);
      wait_vm<10>(); bar(); ST(6 + n);
    }
    wait_vm<0>(); bar(); ST(12);
    tag = __builtin_amdgcn_readfirstlane(*tagw);
  } else {
    tag = __builtin_amdgcn_readfirstlane(__hip_atomic_load((gu32*)(ws + WS_EP + rg * 256), RLX_AGENT)) + 1u;
    if (wv == 4 && lane == 0) *tagw = tag;
    const unsigned r = lane & 31, h2 = lane >> 5;
    unsigned aa[4], wa[2];
#pragma unroll
    for (int s = 0; s < 4; ++s) aa[s] = lb + (b * 32 + r) * 128 + (((2 * s + h2) ^ ((r >> 1) & 7)) << 4);
    { const unsigned blk = (lane >> 4) & 1, q = (lane & 15) >> 2, p = lane & 3;
#pragma unroll
      for (int cb = 0; cb < 2; ++cb) wa[cb] = lb + WT + (8 * h2 + q) * 128 + ((4 * (cb ^ (q >> 1)) + 2 * blk + (p & 1)) << 4) + 8 * (p >> 1); }
    unsigned so = 0;
    f32x16 sacc = {};
    {
      f32x16 acc[6] = {};
      v4u w[6], a;
      bar(); ST(1);
      a = lds128(aa[0]);
#pragma unroll
      for (int t = 0; t < 6; ++t) w[t] = ldstr(wa[t & 1] + (t >> 1) * 8192);
      SB();
#pragma unroll 1
      for (int n = 0; n < 4; ++n) { const unsigned sn = nxt(so); chunk<6, true, true>(acc, w, a, aa, wa, so, sn); so = sn; }
      chunk<6, true, false>(acc, w, a, aa, wa, so, 0);
      ST(2);
      v4u qf[12];
#pragma unroll
      for (int t = 0; t < 6; ++t) { qf[2 * t] = pack8(acc[t], 0); qf[2 * t + 1] = pack8(acc[t], 1); }
      so = nxt(so);
      bar(); ST(3);
#pragma unroll
      for (int k = 0; k < 12; ++k) sacc = mfma32(lds128(aa[k & 3] + so + (k >> 2) * 16384), qf[k], sacc);
    }
    { const unsigned o = WS_SX + ((sgrp * 4 + u) * 4 + b) * 2048;
      __builtin_amdgcn_raw_buffer_store_b128(pack8(sacc, 0), rs, lane * 16, o, 16);
      __builtin_amdgcn_raw_buffer_store_b128(pack8(sacc, 1), rs, lane * 16 + 1024, o, 16); }
    ST(4);
    if (u & 1) { const unsigned t = wa[0]; wa[0] = wa[1]; wa[1] = t + 8192; }
    f32x16 oacc[5] = {};
    v4u pf[2][2];
    {
      v4u w[5], a;
      so = nxt(so);
      bar(); ST(5);
      a = lds128(aa[0] + so);
#pragma unroll
      for (int t = 0; t < 5; ++t) w[t] = ldstr(wa[t & 1] + so + (t >> 1) * 8192);
      wait_vm<0>();
      if (lane == 0) __hip_atomic_store((gu32*)(ws + WS_FS + ((sgrp * 4 + u) * 4 + b) * 128), tag, RLX_AGENT);
      SB();
      {
        f32x16 acc[5] = {};
#pragma unroll 1
        for (int n = 0; n < 3; ++n) { const unsigned sn = nxt(so); chunk<5, false, true>(acc, w, a, aa, wa, so, sn); so = sn; }
        ST(6);
        poll((gu32*)(ws + WS_FS + ((sgrp * 4 + (lane & 3)) * 4 + b) * 128), lane < 4, tag);
        ST(7);
        v4u sp[2][2][2];
#pragma unroll
        for (int e = 0; e < 2; ++e)
#pragma unroll
          for (int d = 0; d < 2; ++d)
#pragma unroll
            for (int s = 0; s < 2; ++s)
              sp[e][d][s] = __builtin_amdgcn_raw_buffer_load_b128(rs, lane * 16 + s * 1024, WS_SX + ((sgrp * 4 + 2 * d + (e ^ hfirst)) * 4 + b) * 2048, 16);
#pragma unroll
        for (int e = 0; e < 2; ++e) {
          f32x16 sv;
#pragma unroll
          for (int s = 0; s < 2; ++s)
#pragma unroll
            for (int j = 0; j < 4; ++j) {
              sv[8 * s + 2 * j] = blo(sp[e][0][s][j]) + blo(sp[e][1][s][j]);
              sv[8 * s + 2 * j + 1] = bhi(sp[e][0][s][j]) + bhi(sp[e][1][s][j]);
            }
          softmax(sv, pf[e]);
        }
        ST(8);
#pragma unroll
        for (int t = 0; t < 5; ++t) { oacc[t] = mfma32(pack8(acc[t], 0), pf[0][0], oacc[t]); oacc[t] = mfma32(pack8(acc[t], 1), pf[0][1], oacc[t]); }
      }
      SB();
      {
        f32x16 acc[5] = {};
#pragma unroll 1
        for (int n = 0; n < 2; ++n) { const unsigned sn = nxt(so); chunk<5, false, true>(acc, w, a, aa, wa, so, sn); so = sn; }
        chunk<5, false, false>(acc, w, a, aa, wa, so, 0);
        ST(9);
#pragma unroll
        for (int t = 0; t < 5; ++t) { oacc[t] = mfma32(pack8(acc[t], 0), pf[1][0], oacc[t]); oacc[t] = mfma32(pack8(acc[t], 1), pf[1][1], oacc[t]); }
      }
    }
    { const unsigned vo = (r >> 3) * (16 * 2560) + h2 * 128 + (r & 7) * 16;
      const unsigned o = WS_X + ((ogrp * 16 + 4 * b) * 16 + member) * 2560;
#pragma unroll
      for (int t = 0; t < 5; ++t) {
        __builtin_amdgcn_raw_buffer_store_b128(pack8(oacc[t], 0), rs, vo + t * 512, o, 16);
        __builtin_amdgcn_raw_buffer_store_b128(pack8(oacc[t], 1), rs, vo + t * 512 + 256, o, 16);
      } }
    ST(10);
    wait_vm<0>();
    if (lane == 0) __hip_atomic_store((gu32*)(ws + WS_FO + ((ogrp * 16 + member) * 4 + b) * 128), tag, RLX_AGENT);
    ST(11);
  }
  if (wv < 5) {
    poll((gu32*)(ws + WS_FO + ((ogrp * 16 + (lane & 15)) * 4 + (member >> 2)) * 128), lane < 16, tag);
    ST(13);
    const unsigned g = lane >> 4, c = lane & 15;
    const unsigned vo = (g >> 1) * 2560 + (g & 1) * 256 + c * 16;
    const unsigned o = WS_X + (ogrp * 16 + member) * (16 * 2560) + wv * 512;
    v4u d[8];
#pragma unroll
    for (int e = 0; e < 8; ++e) d[e] = __builtin_amdgcn_raw_buffer_load_b128(rs, vo, o + e * 5120, 16);
    v4u id;
    { const bool on = (c >> 3) == (g & 1); const unsigned one = (c & 1) ? 0x3f800000u : 0x00003f80u, j2 = (c & 7) >> 1;
      id = (v4u){on && j2 == 0 ? one : 0u, on && j2 == 1 ? one : 0u, on && j2 == 2 ? one : 0u, on && j2 == 3 ? one : 0u}; }
    f32x4 sum = {};
#pragma unroll
    for (int e = 0; e < 8; ++e) sum = mfma16(id, d[e], sum);
    const unsigned row = rg * 128 + member * 8 + (c & 7), col = u * 160 + wv * 32 + (g >> 1) * 16 + (c >> 3) * 8 + (g & 1) * 4;
    *(GAS v2u*)(out + row * 1280 + col * 2) = (v2u){pk(sum[0], sum[1]), pk(sum[2], sum[3])};
    if (wv == 0 && member == 0 && u == 0 && lane == 0) __hip_atomic_store((gu32*)(ws + WS_EP + rg * 256), tag, RLX_AGENT);
    ST(14);
  }
  ST_DUMP();
}

__global__ void amplify(const unsigned long long* st){
  __shared__ unsigned long long smn[256],smx[256],ssum[256];
  int t=threadIdx.x; unsigned long long mn=~0ull,mx=0;
  for(int w=0;w<8;++w){unsigned long long s=st[(t*8+w)*32]; mn=s<mn?s:mn;}
  for(int w=0*4;w<0*4+4;++w){unsigned long long s=st[(t*8+w)*32+0]; mx=s>mx?s:mx;}
  smn[t]=mn;smx[t]=mx;__syncthreads();
  if(t==0){unsigned long long sum=0;for(int i=0;i<256;++i){mn=smn[i]<mn?smn[i]:mn;mx=smx[i]>mx?smx[i]:mx;}
    for(int i=0;i<256;++i)sum+=smx[i]-mn;
    unsigned long long d=0?sum/256:mx-mn; if(d>10000)d=10000; unsigned long long t0=__builtin_amdgcn_s_memrealtime(); while(__builtin_amdgcn_s_memrealtime()-t0<d*100)__builtin_amdgcn_s_sleep(8);}
}
extern "C" void kernel_launch(void* const* d_in, const int* in_sizes, int n_in, void* d_out, int out_size,
                              void* d_ws, size_t ws_size, hipStream_t stream) {
  mha_block_kernel<<<256, 512, 0, stream>>>((const char*)d_in[0], (const char*)d_in[1], (const char*)d_in[2],
                                            (const char*)d_in[3], (const char*)d_in[4], (char*)d_out, (char*)d_ws);
  amplify<<<1,256,0,stream>>>((const unsigned long long*)((char*)d_ws+WS_ST));
}
```

```cpp
#include <hip/hip_runtime.h>
#include <hip/hip_bf16.h>

typedef unsigned v4u __attribute__((ext_vector_type(4)));
typedef unsigned v2u __attribute__((ext_vector_type(2)));
typedef short s16x4 __attribute__((ext_vector_type(4)));
typedef short bf16x8 __attribute__((ext_vector_type(8)));
typedef __bf16 bf2 __attribute__((ext_vector_type(2)));
typedef float f32x2 __attribute__((ext_vector_type(2)));
typedef float f32x4 __attribute__((ext_vector_type(4)));
typedef float f32x16 __attribute__((ext_vector_type(16)));
#define GAS __attribute__((address_space(1)))
#define LAS __attribute__((address_space(3)))
#define DI __device__ __forceinline__
typedef GAS unsigned gu32;
#define RLX_AGENT __ATOMIC_RELAXED, __HIP_MEMORY_SCOPE_AGENT
#define SB() __builtin_amdgcn_sched_barrier(0)

#define STAMP 0
#define ROT 1

constexpr int SLOT = 49152, WT = 16384;
constexpr unsigned WS_EP = 0, WS_FS = 4096, WS_FO = WS_FS + 131072, WS_SX = WS_FO + 131072,
                   WS_X = WS_SX + 256 * 8192, WS_END = WS_X + 16 * 16 * 16 * 2560, WS_ST = 32u << 20;
constexpr int kSpinCap = 1 << 18;

DI f32x16 mfma32(v4u a, v4u b, f32x16 c) {
  return __builtin_amdgcn_mfma_f32_32x32x16_bf16(__builtin_bit_cast(bf16x8, a), __builtin_bit_cast(bf16x8, b), c, 0, 0, 0);
}
DI f32x4 mfma16(v4u a, v4u b, f32x4 c) {
  return __builtin_amdgcn_mfma_f32_16x16x32_bf16(__builtin_bit_cast(bf16x8, a), __builtin_bit_cast(bf16x8, b), c, 0, 0, 0);
}
DI unsigned pk(float a, float b) { f32x2 v = {a, b}; return __builtin_bit_cast(unsigned, __builtin_convertvector(v, bf2)); }
DI v4u pack8(const f32x16& x, int s) {
  return (v4u){pk(x[8 * s], x[8 * s + 1]), pk(x[8 * s + 2], x[8 * s + 3]), pk(x[8 * s + 4], x[8 * s + 5]), pk(x[8 * s + 6], x[8 * s + 7])};
}
DI v4u lds128(unsigned a) { return *(const LAS v4u*)a; }
DI v4u ldstr(unsigned a) {
  s16x4 lo = __builtin_amdgcn_ds_read_tr16_b64_v4i16((LAS s16x4*)a);
  s16x4 hi = __builtin_amdgcn_ds_read_tr16_b64_v4i16((LAS s16x4*)(a + 512));
  return __builtin_bit_cast(v4u, __builtin_shufflevector(lo, hi, 0, 1, 2, 3, 4, 5, 6, 7));
}
template <int LOFF, int GOFF> DI void dma(unsigned voff, const char* sbase, unsigned lbase) {
  asm volatile("s_add_u32 m0, %2, %3\n\ts_nop 0\n\tglobal_load_lds_dwordx4 %0, %1 offset:%4"
               :: "v"(voff), "s"(sbase), "s"(lbase), "i"(LOFF - GOFF), "i"(GOFF) : "memory", "scc");
}
template <int N> DI void wait_vm() { asm volatile("s_waitcnt vmcnt(%0)" :: "i"(N) : "memory"); }
DI void bar() { asm volatile("s_waitcnt lgkmcnt(0)\n\ts_barrier" ::: "memory"); }
DI void poll(gu32* p, bool active, unsigned tag) {
  for (int spins = 0; spins < kSpinCap; ++spins) {
    unsigned f = tag;
    if (active) f = __hip_atomic_load(p, RLX_AGENT);
    if (__all(f == tag)) break;
  }
  __builtin_amdgcn_fence(__ATOMIC_ACQUIRE, "wavefront");
}
#define ST(n)
#define ST_INIT()
#define ST_DUMP()

template <int NT, bool QF, bool NEXT>
DI void chunk(f32x16 (&acc)[NT], v4u (&w)[NT], v4u& a, const unsigned (&aa)[4], const unsigned (&wa)[2], unsigned so, unsigned sn) {
#pragma unroll
  for (int s = 0; s < 3; ++s) {
    v4u an = lds128(aa[s + 1] + so);
#pragma unroll
    for (int t = 0; t < NT; ++t) {
      acc[t] = QF ? mfma32(w[t], a, acc[t]) : mfma32(a, w[t], acc[t]);
      w[t] = ldstr(wa[t & 1] + so + (t >> 1) * 8192 + (s + 1) * 2048);
      SB();
    }
    a = an;
  }
#pragma unroll
  for (int t = 0; t < 3; ++t) acc[t] = QF ? mfma32(w[t], a, acc[t]) : mfma32(a, w[t], acc[t]);
  SB();
  if (NEXT) {
    bar();
    v4u an = lds128(aa[0] + sn);
#pragma unroll
    for (int t = 0; t < 3; ++t) w[t] = ldstr(wa[t & 1] + sn + (t >> 1) * 8192);
    SB();
#pragma unroll
    for (int t = 3; t < NT; ++t) {
      acc[t] = QF ? mfma32(w[t], a, acc[t]) : mfma32(a, w[t], acc[t]);
      w[t] = ldstr(wa[t & 1] + sn + (t >> 1) * 8192);
      SB();
    }
    a = an;
  } else {
#pragma unroll
    for (int t = 3; t < NT; ++t) acc[t] = QF ? mfma32(w[t], a, acc[t]) : mfma32(a, w[t], acc[t]);
  }
}
DI unsigned nxt(unsigned so) { return so == 2 * SLOT ? 0u : so + SLOT; }

DI void softmax(f32x16& s, v4u (&p)[2]) {
  const float c = 0.07216878364870323f * 1.4426950408889634f;
  float m = s[0];
#pragma unroll
  for (int r = 1; r < 16; ++r) m = fmaxf(m, s[r]);
  { auto x = __builtin_amdgcn_permlane32_swap(__float_as_uint(m), __float_as_uint(m), false, false);
    m = fmaxf(__uint_as_float(x[0]), __uint_as_float(x[1])); }
  const float mc = -m * c;
  float l = 0.f;
#pragma unroll
  for (int r = 0; r < 16; ++r) { s[r] = __builtin_amdgcn_exp2f(fmaf(s[r], c, mc)); l += s[r]; }
  { auto x = __builtin_amdgcn_permlane32_swap(__float_as_uint(l), __float_as_uint(l), false, false);
    l = __uint_as_float(x[0]) + __uint_as_float(x[1]); }
  const float rl = __builtin_amdgcn_rcpf(l);
#pragma unroll
  for (int r = 0; r < 16; ++r) s[r] *= rl;
  p[0] = pack8(s, 0); p[1] = pack8(s, 1);
}
DI float blo(unsigned x) { return __uint_as_float(x << 16); }
DI float bhi(unsigned x) { return __uint_as_float(x & 0xffff0000u); }

__global__ void __launch_bounds__(512, 2)
mha_block_kernel(const char* __restrict__ x, const char* __restrict__ kc, const char* __restrict__ vc,
                 const char* __restrict__ wq, const char* __restrict__ wo, char* __restrict__ out, char* ws) {
  __shared__ __attribute__((aligned(1024))) char smem[163840];
  const unsigned lb = (unsigned)(size_t)(LAS char*)smem;
  const int wv = __builtin_amdgcn_readfirstlane(threadIdx.x >> 6), lane = threadIdx.x & 63;
  const int bid = blockIdx.x, xi = bid & 7, r_ = bid >> 3, hp = r_ & 1, u = (r_ >> 1) & 3, rg = r_ >> 3;
  const int b = wv & 3;
  const int hbase = 4 * xi + 2 * hp, dh = u >> 1;
  const int cls1 = ROT ? (2 * hp + (u & 1) + rg) & 3 : 0, cls2 = ROT ? (u + rg) & 3 : 0;
  const int hfirst = cls2 >> 1;
  const int member = 2 * xi + hp, ogrp = rg * 4 + u, sgrp = member * 4 + rg;
  ST_INIT(); ST(0);
  const auto rs = __builtin_amdgcn_make_buffer_rsrc(ws, 0, WS_END, 0x00020000);
  LAS unsigned* tagw = (LAS unsigned*)(lb + 3 * SLOT);
  unsigned tag;

  if (wv < 4) {
    const unsigned l8 = lane >> 3, p = lane & 7, z = lane >> 4;
    const unsigned sw0 = (p ^ z) << 4, sw1 = (p ^ z ^ 4) << 4, sww = (p ^ ((z & 1) << 2)) << 4;
    const unsigned la = lb + b * 4096, lw = lb + WT + b * 2048;
    unsigned va[4], vw[2];
#pragma unroll
    for (int m = 0; m < 4; ++m) va[m] = (8 * m + l8) * 1280 + ((m & 1) ? sw1 : sw0);
#pragma unroll
    for (int m = 0; m < 2; ++m) vw[m] = (8 * m + l8) * 12288 + sww;
    const char* xb = x + (size_t)(rg * 128 + b * 32) * 1280 + dh * 640;
    const char* qb = wq + (size_t)(dh * 320 + b * 16) * 12288 + (hbase + (u & 1)) * 384;
    auto issue = [&](const char* ab, const char* wb, unsigned so) {
      dma<0, 0>(va[0], ab, la + so); dma<1024, 0>(va[1], ab, la + so); dma<2048, 0>(va[2], ab, la + so); dma<3072, 0>(va[3], ab, la + so);
      dma<0, 0>(vw[0], wb, lw + so); dma<8192, 128>(vw[0], wb, lw + so); dma<16384, 256>(vw[0], wb, lw + so);
      dma<1024, 0>(vw[1], wb, lw + so); dma<9216, 128>(vw[1], wb, lw + so); dma<17408, 256>(vw[1], wb, lw + so);
    };
    int c = cls1; unsigned so = 0;
    issue(xb + c * 128, qb + (size_t)c * (64 * 12288), so);
#pragma unroll 1
    for (int n = 1; n < 5; ++n) {
      c = c == 4 ? 0 : c + 1; so = nxt(so);
      issue(xb + c * 128, qb + (size_t)c * (64 * 12288), so);
      wait_vm<10>(); bar(); ST(n);
    }
#pragma unroll
    for (int m = 0; m < 4; ++m) va[m] = (8 * m + l8) * 384 + ((m & 1) ? sw1 : sw0);
#pragma unroll
    for (int m = 0; m < 2; ++m) vw[m] = (8 * m + l8) * 1280 + sww;
    so = nxt(so);
    { const char* kb = kc + (size_t)((rg * 4 + b) * 32 + hbase + (u & 1)) * 12288;
      const char* k0 = kb + dh * 6144; const char* k1 = kb + (dh ^ 1) * 6144;
      const unsigned l0 = la + so + dh * 2048, l1 = la + so + (dh ^ 1) * 2048;
      dma<0, 0>(va[0], k0, l0); dma<16384, 128>(va[0], k0, l0); dma<32768, 256>(va[0], k0, l0);
      dma<1024, 0>(va[1], k0, l0); dma<17408, 128>(va[1], k0, l0); dma<33792, 256>(va[1], k0, l0);
      dma<0, 0>(va[0], k1, l1); dma<16384, 128>(va[0], k1, l1); dma<32768, 256>(va[0], k1, l1);
      dma<1024, 0>(va[1], k1, l1); dma<17408, 128>(va[1], k1, l1); dma<33792, 256>(va[1], k1, l1); }
    wait_vm<12>(); bar(); ST(5);
    const char* vb = vc + (size_t)((rg * 4 + b) * 32 + hbase) * 12288;
    const char* ob = wo + (size_t)(hbase * 192 + b * 16) * 1280 + ((u * 320) & ~127);
#pragma unroll 1
    for (int n = 0; n < 6; ++n) {
      const int hh = (n >= 3) ^ hfirst, t0 = n >= 3 ? n - 3 : n, t = (cls2 & 1) ? 2 - t0 : t0;
      so = nxt(so);
      issue(vb + hh * 12288 + t * 128, ob + (size_t)(hh * 192 + t * 64) * 1280, so);
      wait_vm<10>(); bar(); ST(6 + n);
    }
    wait_vm<0>(); bar(); ST(12);
    tag = __builtin_amdgcn_readfirstlane(*tagw);
  } else {
    tag = __builtin_amdgcn_readfirstlane(__hip_atomic_load((gu32*)(ws + WS_EP + rg * 256), RLX_AGENT)) + 1u;
    if (wv == 4 && lane == 0) *tagw = tag;
    const unsigned r = lane & 31, h2 = lane >> 5;
    unsigned aa[4], wa[2];
#pragma unroll
    for (int s = 0; s < 4; ++s) aa[s] = lb + (b * 32 + r) * 128 + (((2 * s + h2) ^ ((r >> 1) & 7)) << 4);
    { const unsigned blk = (lane >> 4) & 1, q = (lane & 15) >> 2, p = lane & 3;
#pragma unroll
      for (int cb = 0; cb < 2; ++cb) wa[cb] = lb + WT + (8 * h2 + q) * 128 + ((4 * (cb ^ (q >> 1)) + 2 * blk + (p & 1)) << 4) + 8 * (p >> 1); }
    unsigned so = 0;
    f32x16 sacc = {};
    {
      f32x16 acc[6] = {};
      v4u w[6], a;
      bar(); ST(1);
      a = lds128(aa[0]);
#pragma unroll
      for (int t = 0; t < 6; ++t) w[t] = ldstr(wa[t & 1] + (t >> 1) * 8192);
      SB();
#pragma unroll 1
      for (int n = 0; n < 4; ++n) { const unsigned sn = nxt(so); chunk<6, true, true>(acc, w, a, aa, wa, so, sn); so = sn; }
      chunk<6, true, false>(acc, w, a, aa, wa, so, 0);
      ST(2);
      v4u qf[12];
#pragma unroll
      for (int t = 0; t < 6; ++t) { qf[2 * t] = pack8(acc[t], 0); qf[2 * t + 1] = pack8(acc[t], 1); }
      so = nxt(so);
      bar(); ST(3);
#pragma unroll
      for (int k = 0; k < 12; ++k) sacc = mfma32(lds128(aa[k & 3] + so + (k >> 2) * 16384), qf[k], sacc);
    }
    { const unsigned o = WS_SX + ((sgrp * 4 + u) * 4 + b) * 2048;
      __builtin_amdgcn_raw_buffer_store_b128(pack8(sacc, 0), rs, lane * 16, o, 16);
      __builtin_amdgcn_raw_buffer_store_b128(pack8(sacc, 1), rs, lane * 16 + 1024, o, 16); }
    ST(4);
    if (u & 1) { const unsigned t = wa[0]; wa[0] = wa[1]; wa[1] = t + 8192; }
    f32x16 oacc[5] = {};
    v4u pf[2][2];
    {
      v4u w[5], a;
      so = nxt(so);
      bar(); ST(5);
      a = lds128(aa[0] + so);
#pragma unroll
      for (int t = 0; t < 5; ++t) w[t] = ldstr(wa[t & 1] + so + (t >> 1) * 8192);
      wait_vm<0>();
      if (lane == 0) __hip_atomic_store((gu32*)(ws + WS_FS + ((sgrp * 4 + u) * 4 + b) * 128), tag, RLX_AGENT);
      SB();
      {
        f32x16 acc[5] = {};
#pragma unroll 1
        for (int n = 0; n < 3; ++n) { const unsigned sn = nxt(so); chunk<5, false, true>(acc, w, a, aa, wa, so, sn); so = sn; }
        ST(6);
        poll((gu32*)(ws + WS_FS + ((sgrp * 4 + (lane & 3)) * 4 + b) * 128), lane < 4, tag);
        ST(7);
        v4u sp[2][2][2];
#pragma unroll
        for (int e = 0; e < 2; ++e)
#pragma unroll
          for (int d = 0; d < 2; ++d)
#pragma unroll
            for (int s = 0; s < 2; ++s)
              sp[e][d][s] = __builtin_amdgcn_raw_buffer_load_b128(rs, lane * 16 + s * 1024, WS_SX + ((sgrp * 4 + 2 * d + (e ^ hfirst)) * 4 + b) * 2048, 16);
#pragma unroll
        for (int e = 0; e < 2; ++e) {
          f32x16 sv;
#pragma unroll
          for (int s = 0; s < 2; ++s)
#pragma unroll
            for (int j = 0; j < 4; ++j) {
              sv[8 * s + 2 * j] = blo(sp[e][0][s][j]) + blo(sp[e][1][s][j]);
              sv[8 * s + 2 * j + 1] = bhi(sp[e][0][s][j]) + bhi(sp[e][1][s][j]);
            }
          softmax(sv, pf[e]);
        }
        ST(8);
#pragma unroll
        for (int t = 0; t < 5; ++t) { oacc[t] = mfma32(pack8(acc[t], 0), pf[0][0], oacc[t]); oacc[t] = mfma32(pack8(acc[t], 1), pf[0][1], oacc[t]); }
      }
      SB();
      {
        f32x16 acc[5] = {};
#pragma unroll 1
        for (int n = 0; n < 2; ++n) { const unsigned sn = nxt(so); chunk<5, false, true>(acc, w, a, aa, wa, so, sn); so = sn; }
        chunk<5, false, false>(acc, w, a, aa, wa, so, 0);
        ST(9);
#pragma unroll
        for (int t = 0; t < 5; ++t) { oacc[t] = mfma32(pack8(acc[t], 0), pf[1][0], oacc[t]); oacc[t] = mfma32(pack8(acc[t], 1), pf[1][1], oacc[t]); }
      }
    }
    { const unsigned vo = (r >> 3) * (16 * 2560) + h2 * 128 + (r & 7) * 16;
      const unsigned o = WS_X + ((ogrp * 16 + 4 * b) * 16 + member) * 2560;
#pragma unroll
      for (int t = 0; t < 5; ++t) {
        __builtin_amdgcn_raw_buffer_store_b128(pack8(oacc[t], 0), rs, vo + t * 512, o, 16);
        __builtin_amdgcn_raw_buffer_store_b128(pack8(oacc[t], 1), rs, vo + t * 512 + 256, o, 16);
      } }
    ST(10);
    wait_vm<0>();
    if (lane == 0) __hip_atomic_store((gu32*)(ws + WS_FO + ((ogrp * 16 + member) * 4 + b) * 128), tag, RLX_AGENT);
    ST(11);
  }
  if (wv < 5) {
    poll((gu32*)(ws + WS_FO + ((ogrp * 16 + (lane & 15)) * 4 + (member >> 2)) * 128), lane < 16, tag);
    ST(13);
    const unsigned g = lane >> 4, c = lane & 15;
    const unsigned vo = (g >> 1) * 2560 + (g & 1) * 256 + c * 16;
    const unsigned o = WS_X + (ogrp * 16 + member) * (16 * 2560) + wv * 512;
    v4u d[8];
#pragma unroll
    for (int e = 0; e < 8; ++e) d[e] = __builtin_amdgcn_raw_buffer_load_b128(rs, vo, o + e * 5120, 16);
    v4u id;
    { const bool on = (c >> 3) == (g & 1); const unsigned one = (c & 1) ? 0x3f800000u : 0x00003f80u, j2 = (c & 7) >> 1;
      id = (v4u){on && j2 == 0 ? one : 0u, on && j2 == 1 ? one : 0u, on && j2 == 2 ? one : 0u, on && j2 == 3 ? one : 0u}; }
    f32x4 sum = {};
#pragma unroll
    for (int e = 0; e < 8; ++e) sum = mfma16(id, d[e], sum);
    const unsigned row = rg * 128 + member * 8 + (c & 7), col = u * 160 + wv * 32 + (g >> 1) * 16 + (c >> 3) * 8 + (g & 1) * 4;
    *(GAS v2u*)(out + row * 1280 + col * 2) = (v2u){pk(sum[0], sum[1]), pk(sum[2], sum[3])};
    if (wv == 0 && member == 0 && u == 0 && lane == 0) __hip_atomic_store((gu32*)(ws + WS_EP + rg * 256), tag, RLX_AGENT);
    ST(14);
  }
  ST_DUMP();
}

extern "C" void kernel_launch(void* const* d_in, const int* in_sizes, int n_in, void* d_out, int out_size,
                              void* d_ws, size_t ws_size, hipStream_t stream) {
  mha_block_kernel<<<256, 512, 0, stream>>>((const char*)d_in[0], (const char*)d_in[1], (const char*)d_in[2],
                                            (const char*)d_in[3], (const char*)d_in[4], (char*)d_out, (char*)d_ws);
}
```

```cpp
#include <hip/hip_runtime.h>
#include <hip/hip_bf16.h>

typedef unsigned v4u __attribute__((ext_vector_type(4)));
typedef unsigned v2u __attribute__((ext_vector_type(2)));
typedef short s16x4 __attribute__((ext_vector_type(4)));
typedef short bf16x8 __attribute__((ext_vector_type(8)));
typedef __bf16 bf2 __attribute__((ext_vector_type(2)));
typedef float f32x2 __attribute__((ext_vector_type(2)));
typedef float f32x4 __attribute__((ext_vector_type(4)));
typedef float f32x16 __attribute__((ext_vector_type(16)));
#define GAS __attribute__((address_space(1)))
#define LAS __attribute__((address_space(3)))
#define DI __device__ __forceinline__
typedef GAS unsigned gu32;
#define RLX_AGENT __ATOMIC_RELAXED, __HIP_MEMORY_SCOPE_AGENT
#define SB() __builtin_amdgcn_sched_barrier(0)

#define STAMP 0
#define ROT 3
#define THR 0
#define NFL 2

constexpr int SLOT = 49152, WT = 16384;
constexpr unsigned WS_EP = 0, WS_FO = 4096, WS_SX = WS_FO + 5 * 131072,
                   WS_X = WS_SX + 256 * 12288, WS_END = WS_X + 16 * 16 * 16 * 2560, WS_ST = 32u << 20;
constexpr int kSpinCap = 1 << 18;

DI f32x16 mfma32(v4u a, v4u b, f32x16 c) {
  return __builtin_amdgcn_mfma_f32_32x32x16_bf16(__builtin_bit_cast(bf16x8, a), __builtin_bit_cast(bf16x8, b), c, 0, 0, 0);
}
DI f32x4 mfma16(v4u a, v4u b, f32x4 c) {
  return __builtin_amdgcn_mfma_f32_16x16x32_bf16(__builtin_bit_cast(bf16x8, a), __builtin_bit_cast(bf16x8, b), c, 0, 0, 0);
}
DI unsigned pk(float a, float b) { f32x2 v = {a, b}; return __builtin_bit_cast(unsigned, __builtin_convertvector(v, bf2)); }
DI v4u pack8(const f32x16& x, int s) {
  return (v4u){pk(x[8 * s], x[8 * s + 1]), pk(x[8 * s + 2], x[8 * s + 3]), pk(x[8 * s + 4], x[8 * s + 5]), pk(x[8 * s + 6], x[8 * s + 7])};
}
DI v4u lds128(unsigned a) { return *(const LAS v4u*)a; }
DI v4u ldstr(unsigned a) {
  s16x4 lo = __builtin_amdgcn_ds_read_tr16_b64_v4i16((LAS s16x4*)a);
  s16x4 hi = __builtin_amdgcn_ds_read_tr16_b64_v4i16((LAS s16x4*)(a + 512));
  return __builtin_bit_cast(v4u, __builtin_shufflevector(lo, hi, 0, 1, 2, 3, 4, 5, 6, 7));
}
template <int LOFF, int GOFF> DI void dma(unsigned voff, const char* sbase, unsigned lbase) {
  asm volatile("s_add_u32 m0, %2, %3\n\ts_nop 0\n\tglobal_load_lds_dwordx4 %0, %1 offset:%4"
               :: "v"(voff), "s"(sbase), "s"(lbase), "i"(LOFF - GOFF), "i"(GOFF) : "memory", "scc");
}
template <int N> DI void wait_vm() { asm volatile("s_waitcnt vmcnt(%0)" :: "i"(N) : "memory"); }
DI void bar() { asm volatile("s_waitcnt lgkmcnt(0)\n\ts_barrier" ::: "memory"); }
DI void poll(gu32* p, bool active, unsigned tag) {
  for (int spins = 0; spins < kSpinCap; ++spins) {
    unsigned f = tag;
    if (active) f = __hip_atomic_load(p, RLX_AGENT);
    if (__all(f == tag)) break;
    __builtin_amdgcn_s_sleep(1);
  }
  __builtin_amdgcn_fence(__ATOMIC_ACQUIRE, "wavefront");
}
#define ST(n)
#define ST_INIT()
#define ST_DUMP()

DI unsigned opq(unsigned x) { asm volatile("" : "+v"(x)); return x; }
template <int NT, bool QF, bool NEXT>
DI void chunk(f32x16 (&acc)[NT], v4u (&w)[NT], v4u& a, const unsigned (&aa)[4], const unsigned (&wa)[2], unsigned (&wc)[2], unsigned so, unsigned sn) {
#pragma unroll
  for (int s = 0; s < 3; ++s) {
    v4u an = lds128(aa[s + 1] + so);
#pragma unroll
    for (int t = 0; t < NT; ++t) {
      acc[t] = QF ? mfma32(w[t], a, acc[t]) : mfma32(a, w[t], acc[t]);
      w[t] = ldstr(wc[t & 1] + (t >> 1) * 8192 + (s + 1) * 2048);
      SB();
    }
    a = an;
  }
  unsigned a0;
  if (NEXT) { wc[0] = opq(wa[0] + sn); wc[1] = opq(wa[1] + sn); a0 = opq(aa[0] + sn); }
#pragma unroll
  for (int t = 0; t < NT - 3; ++t) acc[t] = QF ? mfma32(w[t], a, acc[t]) : mfma32(a, w[t], acc[t]);
  SB();
  if (NEXT) {
    bar();
    v4u an = lds128(a0);
    w[0] = ldstr(wc[0]);
    SB();
#pragma unroll
    for (int t = NT - 3; t < NT; ++t) {
      acc[t] = QF ? mfma32(w[t], a, acc[t]) : mfma32(a, w[t], acc[t]);
      const int e = t - (NT - 3) + 1;
      if (e < NT - 3) w[e] = ldstr(wc[e & 1] + (e >> 1) * 8192);
      w[t] = ldstr(wc[t & 1] + (t >> 1) * 8192);
      SB();
    }
    a = an;
  } else {
#pragma unroll
    for (int t = NT - 3; t < NT; ++t) acc[t] = QF ? mfma32(w[t], a, acc[t]) : mfma32(a, w[t], acc[t]);
  }
}
DI unsigned nxt(unsigned so) { return so == 2 * SLOT ? 0u : so + SLOT; }
template <int NT> DI void zero_acc(f32x16 (&acc)[NT]) {
#pragma unroll
  for (int t = 0; t < NT; ++t) { acc[t] = (f32x16){}; asm volatile("" : "+v"(acc[t])); }
}

DI void softmax(f32x16& s, v4u (&p)[2]) {
  const float c = 0.07216878364870323f * 1.4426950408889634f;
  float m = s[0];
#pragma unroll
  for (int r = 1; r < 16; ++r) m = fmaxf(m, s[r]);
  { auto x = __builtin_amdgcn_permlane32_swap(__float_as_uint(m), __float_as_uint(m), false, false);
    m = fmaxf(__uint_as_float(x[0]), __uint_as_float(x[1])); }
  const float mc = -m * c;
  float l = 0.f;
#pragma unroll
  for (int r = 0; r < 16; ++r) { s[r] = __builtin_amdgcn_exp2f(fmaf(s[r], c, mc)); l += s[r]; }
  { auto x = __builtin_amdgcn_permlane32_swap(__float_as_uint(l), __float_as_uint(l), false, false);
    l = __uint_as_float(x[0]) + __uint_as_float(x[1]); }
  const float rl = __builtin_amdgcn_rcpf(l);
#pragma unroll
  for (int r = 0; r < 16; ++r) s[r] *= rl;
  p[0] = pack8(s, 0); p[1] = pack8(s, 1);
}
DI float blo(unsigned x) { return __uint_as_float(x << 16); }
DI float bhi(unsigned x) { return __uint_as_float(x & 0xffff0000u); }
DI float sval(const v4u (&g)[3], int r) {
  const int q = r / 3, e = r % 3;
  const unsigned d = g[q >> 1][(q & 1) * 2 + (e == 2)];
  return e == 1 ? bhi(d) : blo(d);
}
template <int OFF> DI void ld_sc1(v4u& d, unsigned voff, const char* sbase) {
  asm volatile("global_load_dwordx4 %0, %1, %2 offset:%3 sc1" : "=v"(d) : "v"(voff), "s"(sbase), "i"(OFF) : "memory");
}
template <int OFF> DI void st_sc1(v4u d, unsigned voff, char* sbase) {
  asm volatile("global_store_dwordx4 %1, %0, %2 offset:%3 sc1\n\ts_nop 1" :: "v"(d), "v"(voff), "s"(sbase), "i"(OFF) : "memory");
}

__global__ void __launch_bounds__(512, 2)
mha_block_kernel(const char* __restrict__ x, const char* __restrict__ kc, const char* __restrict__ vc,
                 const char* __restrict__ wq, const char* __restrict__ wo, char* __restrict__ out, char* ws) {
  __shared__ __attribute__((aligned(1024))) char smem[163840];
  asm volatile("" :: "s"(x), "s"(kc), "s"(vc), "s"(wq), "s"(wo), "s"(out), "s"(ws));
  const unsigned lb = (unsigned)(size_t)(LAS char*)smem;
  const int wv = __builtin_amdgcn_readfirstlane(threadIdx.x >> 6), lane = threadIdx.x & 63;
  const int bid = blockIdx.x, xi = bid & 7, r_ = bid >> 3, hp = r_ & 1, u = (r_ >> 1) & 3, rg = r_ >> 3;
  const int b = wv & 3;
  const int hbase = 4 * xi + 2 * hp, dh = u >> 1;
  const int member = 2 * xi + hp, ogrp = rg * 4 + u, sgrp = member * 4 + rg;
  ST_INIT(); ST(0);
  const auto rs = __builtin_amdgcn_make_buffer_rsrc(ws, 0, WS_END, 0x00020000);
  LAS unsigned* tagw = (LAS unsigned*)(lb + 40960);
  const unsigned sp = lb + 3 * SLOT + b * 4096 + lane * 16;
  unsigned tag;

  if (__builtin_expect(wv < 4, 1)) {
    const unsigned l8 = lane >> 3, p = lane & 7, z = lane >> 4;
    const unsigned sw0 = (p ^ z) << 4, sw1 = (p ^ z ^ 4) << 4, sww = (p ^ ((z & 1) << 2)) << 4;
    unsigned va0 = l8 * 1280 + sw0, va1 = (8 + l8) * 1280 + sw1, vw = l8 * 12288 + sww;
    const int cls1 = ((2 * hp + (u & 1)) ^ rg) & ROT, cls2 = (u ^ rg) & ROT;
    auto half = [&](const char* ab, const char* wb, unsigned la, unsigned lw) {
      if (THR) wait_vm<THR>();
      dma<0, 0>(va0, ab, la); dma<1024, 0>(va1, ab, la);
      if (THR) wait_vm<THR>();
      dma<0, 0>(vw, wb, lw); dma<8192, 128>(vw, wb, lw); dma<16384, 256>(vw, wb, lw);
    };
    unsigned so = 0;
    {
      const int c0 = cls1 & 1, c1 = cls1 >> 1;
      const char* xb = x + (size_t)(rg * 128 + b * 32) * 1280 + dh * 640;
      const char* qb = wq + (size_t)(dh * 320 + b * 16) * 12288 + (hbase + (u & 1)) * 384;
      const char *xb0 = xb + c0 * (16 * 1280), *xb1 = xb + (c0 ^ 1) * (16 * 1280), *qb0 = qb + c0 * (8 * 12288), *qb1 = qb + (c0 ^ 1) * (8 * 12288);
      const unsigned la0 = lb + b * 4096 + c0 * 2048, la1 = la0 ^ 2048, lw0 = lb + WT + b * 2048 + c0 * 1024, lw1 = lw0 ^ 1024;
#pragma unroll 1
      for (int n = 0; n < 5; ++n) {
        const int m = n - dh, c = (unsigned)m < 4u ? m ^ c1 : 4;
        half(xb0 + c * 128, qb0 + (size_t)c * (64 * 12288), la0 + so, lw0 + so);
        half(xb1 + c * 128, qb1 + (size_t)c * (64 * 12288), la1 + so, lw1 + so);
        so = nxt(so);
        if (n) { wait_vm<10>(); bar(); ST(n); }
      }
    }
    va0 = l8 * 384 + sw0; va1 = (8 + l8) * 384 + sw1; vw = l8 * 1280 + sww;
    { const char* kb = kc + (size_t)((rg * 4 + b) * 32 + hbase + (u & 1)) * 12288;
      const char* k0 = kb + dh * 6144; const char* k1 = kb + (dh ^ 1) * 6144;
      const unsigned l0 = lb + b * 4096 + so + dh * 2048, l1 = l0 ^ 2048;
      if (THR) wait_vm<THR>();
      dma<0, 0>(va0, k0, l0); dma<16384, 128>(va0, k0, l0); dma<32768, 256>(va0, k0, l0);
      if (THR) wait_vm<THR>();
      dma<1024, 0>(va1, k0, l0); dma<17408, 128>(va1, k0, l0); dma<33792, 256>(va1, k0, l0);
      if (THR) wait_vm<THR>();
      dma<0, 0>(va0, k1, l1); dma<16384, 128>(va0, k1, l1); dma<32768, 256>(va0, k1, l1);
      if (THR) wait_vm<THR>();
      dma<1024, 0>(va1, k1, l1); dma<17408, 128>(va1, k1, l1); dma<33792, 256>(va1, k1, l1); }
    so = nxt(so);
    wait_vm<12>(); bar(); ST(5);
    tag = __builtin_amdgcn_readfirstlane(*tagw);
    const int c0 = cls2 & 1, c1 = cls2 >> 1;
    const char* vb = vc + (size_t)((rg * 4 + b) * 32 + hbase) * 12288;
    const char* ob = wo + (size_t)(hbase * 192 + b * 16) * 1280 + ((u * 320) & ~127);
    const char *vb0 = vb + c0 * (16 * 384), *vb1 = vb + (c0 ^ 1) * (16 * 384), *ob0 = ob + c0 * (8 * 1280), *ob1 = ob + (c0 ^ 1) * (8 * 1280);
    const unsigned la0 = lb + b * 4096 + c0 * 2048, la1 = la0 ^ 2048, lw0 = lb + WT + b * 2048 + c0 * 1024, lw1 = lw0 ^ 1024;
    auto vchunk = [&](int hh, int n) {
      const int m = n - hp, t = (unsigned)m < 2u ? m ^ c1 : 2;
      half(vb0 + hh * 12288 + t * 128, ob0 + (hh * 192 + t * 64) * 1280, la0 + so, lw0 + so);
      half(vb1 + hh * 12288 + t * 128, ob1 + (hh * 192 + t * 64) * 1280, la1 + so, lw1 + so);
      so = nxt(so);
    };
    vchunk(0, 0); wait_vm<10>(); bar(); ST(6);
    vchunk(0, 1); wait_vm<10>(); bar(); ST(7);
    f32x16 sown;
    const unsigned t16 = tag << 16;
    char* sx = ws + WS_SX + (sgrp * 16 + b) * 3072;
    { const f32x4 s0 = *(const LAS f32x4*)sp, s1 = *(const LAS f32x4*)(sp + 1024), s2 = *(const LAS f32x4*)(sp + 2048), s3 = *(const LAS f32x4*)(sp + 3072);
#pragma unroll
      for (int j = 0; j < 4; ++j) { sown[j] = s0[j]; sown[4 + j] = s1[j]; sown[8 + j] = s2[j]; sown[12 + j] = s3[j]; }
      char* mine = sx + u * 12288;
      st_sc1<0>((v4u){pk(sown[0], sown[1]), pk(sown[2], 0.f) | t16, pk(sown[3], sown[4]), pk(sown[5], 0.f) | t16}, lane * 16, mine);
      st_sc1<1024>((v4u){pk(sown[6], sown[7]), pk(sown[8], 0.f) | t16, pk(sown[9], sown[10]), pk(sown[11], 0.f) | t16}, lane * 16, mine);
      st_sc1<2048>((v4u){pk(sown[12], sown[13]), pk(sown[14], 0.f) | t16, pk(sown[15], 0.f), t16}, lane * 16, mine); }
    vchunk(0, 2); wait_vm<13>(); bar(); ST(8);
    vchunk(1, 0); wait_vm<10>(); bar(); ST(9);
    v4u g[3][3];
    auto gather = [&]() {
#pragma unroll
      for (int e = 0; e < 3; ++e) { const char* src = sx + (u ^ (e == 0 ? 2 : e == 1 ? 1 : 3)) * 12288;
        ld_sc1<0>(g[e][0], lane * 16, src); ld_sc1<1024>(g[e][1], lane * 16, src); ld_sc1<2048>(g[e][2], lane * 16, src); }
    };
#define PIN_G() asm volatile("" : "+v"(g[0][0]), "+v"(g[0][1]), "+v"(g[0][2]), "+v"(g[1][0]), "+v"(g[1][1]), "+v"(g[1][2]), "+v"(g[2][0]), "+v"(g[2][1]), "+v"(g[2][2]))
    gather();
    vchunk(1, 1); wait_vm<19>(); bar(); ST(10);
    vchunk(1, 2); wait_vm<20>(); PIN_G();
    ST(16);
    for (int spins = 0; spins < kSpinCap; ++spins) {
      unsigned bad = 0;
#pragma unroll
      for (int e = 0; e < 3; ++e)
#pragma unroll
        for (int j = 0; j < 3; ++j) bad |= (g[e][j][1] ^ t16) | (g[e][j][3] ^ t16);
      if (__all(bad < 0x10000u)) break;
      gather(); wait_vm<0>(); PIN_G();
    }
    ST(17);
    { v4u pf[2];
#pragma unroll
      for (int r = 0; r < 16; ++r) sown[r] += sval(g[0], r);
      softmax(sown, pf);
      *(LAS v4u*)(sp + (u & 1) * 2048) = pf[0]; *(LAS v4u*)(sp + (u & 1) * 2048 + 1024) = pf[1]; }
    ST(18);
    wait_vm<10>(); bar(); ST(11);
    PIN_G();
    { v4u pf[2]; f32x16 sv;
#pragma unroll
      for (int r = 0; r < 16; ++r) sv[r] = sval(g[1], r) + sval(g[2], r);
      softmax(sv, pf);
      *(LAS v4u*)(sp + ((u & 1) ^ 1) * 2048) = pf[0]; *(LAS v4u*)(sp + ((u & 1) ^ 1) * 2048 + 1024) = pf[1]; }
    ST(19);
    wait_vm<0>(); bar(); ST(12);
    bar();
  } else {
    tag = __builtin_amdgcn_readfirstlane(__hip_atomic_load((gu32*)(ws + WS_EP + rg * 256), RLX_AGENT)) + 1u;
    if (wv == 4 && lane == 0) *tagw = tag;
    const unsigned r = lane & 31, h2 = lane >> 5;
    unsigned aa[4], wa[2], wc[2];
#pragma unroll
    for (int s = 0; s < 4; ++s) aa[s] = opq(lb + (b * 32 + r) * 128 + (((2 * s + h2) ^ ((r >> 1) & 7)) << 4));
    { const unsigned blk = (lane >> 4) & 1, q = (lane & 15) >> 2, p = lane & 3;
#pragma unroll
      for (int cb = 0; cb < 2; ++cb) wc[cb] = wa[cb] = opq(lb + WT + (8 * h2 + q) * 128 + ((4 * (cb ^ (q >> 1)) + 2 * blk + (p & 1)) << 4) + 8 * (p >> 1)); }
    unsigned so = 0;
    {
      f32x16 acc[6];
      zero_acc(acc);
      v4u w[6], a;
      bar(); ST(1);
      a = lds128(aa[0]);
#pragma unroll
      for (int t = 0; t < 6; ++t) { w[t] = ldstr(wa[t & 1] + (t >> 1) * 8192); SB(); }
#pragma unroll 1
      for (int n = 0; n < 4; ++n) { const unsigned sn = nxt(so); chunk<6, true, true>(acc, w, a, aa, wa, wc, so, sn); so = sn; }
      chunk<6, true, false>(acc, w, a, aa, wa, wc, so, 0);
      ST(2);
      bar(); ST(3);
      f32x16 sacc = {};
      unsigned ka[4];
#pragma unroll
      for (int s = 0; s < 4; ++s) ka[s] = opq(aa[s] + 2 * SLOT);
      v4u kf[12];
#pragma unroll
      for (int k = 0; k < 12; ++k) kf[k] = lds128(ka[k & 3] + (k >> 2) * 16384);
#pragma unroll
      for (int k = 0; k < 12; ++k) sacc = mfma32(kf[k], pack8(acc[k >> 1], k & 1), sacc);
#pragma unroll
      for (int j = 0; j < 4; ++j) *(LAS f32x4*)(sp + j * 1024) = (f32x4){sacc[4 * j], sacc[4 * j + 1], sacc[4 * j + 2], sacc[4 * j + 3]};
    }
    ST(4);
    if (u & 1) { const unsigned t = wa[0]; wa[0] = wa[1]; wa[1] = t + 8192; }
    so = 0;
    wc[0] = wa[0] = opq(wa[0]); wc[1] = wa[1] = opq(wa[1]);
    v4u w[5], a, vw0[5][2];
    f32x16 acc[5];
    zero_acc(acc);
    bar(); ST(5);
    a = lds128(aa[0]);
#pragma unroll
    for (int t = 0; t < 5; ++t) { w[t] = ldstr(wc[t & 1] + (t >> 1) * 8192); SB(); }
#pragma unroll 1
    for (int n = 0; n < 3; ++n) { const unsigned sn = nxt(so); chunk<5, false, true>(acc, w, a, aa, wa, wc, so, sn); so = sn; }
    ST(6);
#pragma unroll
    for (int t = 0; t < 5; ++t) { vw0[t][0] = pack8(acc[t], 0); vw0[t][1] = pack8(acc[t], 1); }
    zero_acc(acc);
    ST(7);
#pragma unroll 1
    for (int n = 0; n < 2; ++n) { const unsigned sn = nxt(so); chunk<5, false, true>(acc, w, a, aa, wa, wc, so, sn); so = sn; }
    v4u pf[4];
#pragma unroll
    for (int j = 0; j < 4; ++j) pf[j] = lds128(sp + j * 1024);
    chunk<5, false, false>(acc, w, a, aa, wa, wc, so, 0);
    ST(9);
    { const unsigned vo = (r >> 3) * (16 * 2560) + h2 * 128 + (r & 7) * 16;
      const unsigned o = WS_X + ((ogrp * 16 + 4 * b) * 16 + member) * 2560;
#pragma unroll
      for (int t = 0; t < 5; ++t) {
        f32x16 oacc = {};
        oacc = mfma32(vw0[t][0], pf[0], oacc); oacc = mfma32(vw0[t][1], pf[1], oacc);
        oacc = mfma32(pack8(acc[t], 0), pf[2], oacc); oacc = mfma32(pack8(acc[t], 1), pf[3], oacc);
        __builtin_amdgcn_raw_buffer_store_b128(pack8(oacc, 0), rs, vo + t * 512, o, 16);
        __builtin_amdgcn_raw_buffer_store_b128(pack8(oacc, 1), rs, vo + t * 512 + 256, o, 16);
      } }
    ST(10);
    bar();
    gu32* fl = (gu32*)(ws + WS_FO + ((ogrp * 16 + member) * 4 + b) * 128);
    if (NFL == 1) { wait_vm<0>(); if (lane == 0) __hip_atomic_store(fl, tag, RLX_AGENT); }
    if (NFL == 2) {
      wait_vm<4>(); if (lane == 0) __hip_atomic_store(fl, tag, RLX_AGENT);
      wait_vm<1>(); if (lane == 0) __hip_atomic_store(fl + 32768, tag, RLX_AGENT);
    }
    if (NFL == 5) {
      wait_vm<8>(); if (lane == 0) __hip_atomic_store(fl, tag, RLX_AGENT);
      wait_vm<7>(); if (lane == 0) __hip_atomic_store(fl + 32768, tag, RLX_AGENT);
      wait_vm<6>(); if (lane == 0) __hip_atomic_store(fl + 2 * 32768, tag, RLX_AGENT);
      wait_vm<5>(); if (lane == 0) __hip_atomic_store(fl + 3 * 32768, tag, RLX_AGENT);
      wait_vm<4>(); if (lane == 0) __hip_atomic_store(fl + 4 * 32768, tag, RLX_AGENT);
    }
    ST(11);
  }
  if (wv < 5) {
    const int fsel = NFL == 1 ? 0 : NFL == 2 ? (wv >= 3) : wv;
    poll((gu32*)(ws + WS_FO + fsel * 131072 + ((ogrp * 16 + (lane & 15)) * 4 + (member >> 2)) * 128), lane < 16, tag);
    ST(13);
    const unsigned g = lane >> 4, c = lane & 15;
    const unsigned vo = (g >> 1) * 2560 + (g & 1) * 256 + c * 16;
    const unsigned o = WS_X + (ogrp * 16 + member) * (16 * 2560) + wv * 512;
    v4u d[8];
#pragma unroll
    for (int e = 0; e < 8; ++e) d[e] = __builtin_amdgcn_raw_buffer_load_b128(rs, vo, o + e * 5120, 16);
    v4u id;
    { const bool on = (c >> 3) == (g & 1); const unsigned one = (c & 1) ? 0x3f800000u : 0x00003f80u, j2 = (c & 7) >> 1;
      id = (v4u){on && j2 == 0 ? one : 0u, on && j2 == 1 ? one : 0u, on && j2 == 2 ? one : 0u, on && j2 == 3 ? one : 0u}; }
    f32x4 sum = {};
#pragma unroll
    for (int e = 0; e < 8; ++e) sum = mfma16(id, d[e], sum);
    const unsigned row = rg * 128 + member * 8 + (c & 7), col = u * 160 + wv * 32 + (g >> 1) * 16 + (c >> 3) * 8 + (g & 1) * 4;
    *(GAS v2u*)(out + row * 1280 + col * 2) = (v2u){pk(sum[0], sum[1]), pk(sum[2], sum[3])};
    if (wv == 0 && member == 0 && u == 0 && lane == 0) __hip_atomic_store((gu32*)(ws + WS_EP + rg * 256), tag, RLX_AGENT);
    ST(14);
  }
  ST_DUMP();
}

extern "C" void kernel_launch(void* const* d_in, const int* in_sizes, int n_in, void* d_out, int out_size,
                              void* d_ws, size_t ws_size, hipStream_t stream) {
  mha_block_kernel<<<256, 512, 0, stream>>>((const char*)d_in[0], (const char*)d_in[1], (const char*)d_in[2],
                                            (const char*)d_in[3], (const char*)d_in[4], (char*)d_out, (char*)d_ws);
}
```

```cpp
#include <hip/hip_runtime.h>
#include <hip/hip_bf16.h>

typedef unsigned v4u __attribute__((ext_vector_type(4)));
typedef unsigned v2u __attribute__((ext_vector_type(2)));
typedef short s16x4 __attribute__((ext_vector_type(4)));
typedef short bf16x8 __attribute__((ext_vector_type(8)));
typedef __bf16 bf2 __attribute__((ext_vector_type(2)));
typedef float f32x2 __attribute__((ext_vector_type(2)));
typedef float f32x4 __attribute__((ext_vector_type(4)));
#define GAS __attribute__((address_space(1)))
#define LAS __attribute__((address_space(3)))
#define DI __device__ __forceinline__
typedef GAS unsigned gu32;
#define RLX_AGENT __ATOMIC_RELAXED, __HIP_MEMORY_SCOPE_AGENT
#define SB() __builtin_amdgcn_sched_barrier(0)

#define STAMP 0
#define QS 1
#define VS 0
#define XM 0
#define HINT 2

constexpr int SLOT = 40960, WT = 16384, SPARE = 3 * SLOT;
constexpr unsigned WS_EP = 0, WS_FO = 4096, WS_P = WS_FO + 512 * 128, WS_X = WS_P + 512 * 2048,
                   WS_END = WS_X + 128 * 8 * 5120, WS_ST = 32u << 20;
constexpr int kSpinCap = 1 << 18;

DI f32x4 mfma(v4u a, v4u b, f32x4 c) {
  return __builtin_amdgcn_mfma_f32_16x16x32_bf16(__builtin_bit_cast(bf16x8, a), __builtin_bit_cast(bf16x8, b), c, 0, 0, 0);
}
DI unsigned pk(float a, float b) { f32x2 v = {a, b}; return __builtin_bit_cast(unsigned, __builtin_convertvector(v, bf2)); }
DI v4u pack2(const f32x4& x, const f32x4& y) { return (v4u){pk(x[0], x[1]), pk(x[2], x[3]), pk(y[0], y[1]), pk(y[2], y[3])}; }
DI v4u lds128(unsigned a) { return *(const LAS v4u*)a; }
DI v2u lds64(unsigned a) { return *(const LAS v2u*)a; }
DI v4u ldstr(unsigned a) {
  s16x4 lo = __builtin_amdgcn_ds_read_tr16_b64_v4i16((LAS s16x4*)a);
  s16x4 hi = __builtin_amdgcn_ds_read_tr16_b64_v4i16((LAS s16x4*)(a + 512));
  return __builtin_bit_cast(v4u, __builtin_shufflevector(lo, hi, 0, 1, 2, 3, 4, 5, 6, 7));
}
template <int LOFF, int GOFF> DI void dma(unsigned voff, const char* sbase, unsigned lbase) {
  asm volatile("s_add_u32 m0, %2, %3\n\ts_nop 0\n\tglobal_load_lds_dwordx4 %0, %1 offset:%4"
               :: "v"(voff), "s"(sbase), "s"(lbase), "i"(LOFF - GOFF), "i"(GOFF) : "memory", "scc");
}
template <int N> DI void wait_vm() { asm volatile("s_waitcnt vmcnt(%0)" :: "i"(N) : "memory"); }
DI void bar() { asm volatile("s_waitcnt lgkmcnt(0)\n\ts_barrier" ::: "memory"); }
DI unsigned opq(unsigned x) { asm volatile("" : "+v"(x)); return x; }
DI unsigned nxt(unsigned so) { return so == 2 * SLOT ? 0u : so + SLOT; }
DI void poll(gu32* p, bool active, unsigned tag) {
  for (int spins = 0; spins < kSpinCap; ++spins) {
    unsigned f = tag;
    if (active) f = __hip_atomic_load(p, RLX_AGENT);
    if (__all(f == tag)) break;
    __builtin_amdgcn_s_sleep(1);
  }
  __builtin_amdgcn_fence(__ATOMIC_ACQUIRE, "wavefront");
}
DI v4u tag_bits(unsigned epoch, int lane) {
  v4u m;
#pragma unroll
  for (int h = 0; h < 2; ++h) {
    const unsigned e = ((epoch >> (3 + 3 * h + 6 * (lane & 3))) + epoch) & 7u;
    m[2 * h] = (~e & 1u) | ((e & 1u) << 16);
    m[2 * h + 1] = ((e >> 1) & 1u) | ((e >> 2) << 16);
  }
  return m;
}
#define ST(n)
#define ST_INIT()
#define ST_DUMP()

constexpr unsigned long long sched_q(int c1, int cls) {
  unsigned long long s = 0;
  for (int n = 0; n < 10; ++n) {
    const int c = QS == 0 ? n : QS == 1 ? n ^ c1 : QS == 2 ? (n + 5 * c1) % 10 : (n + (5 * cls) / 2) % 10;
    s |= (unsigned long long)c << (4 * n);
  }
  return s;
}
constexpr unsigned long long sched_v(int c1, int cls) {
  unsigned long long s = 0;
  for (int n = 0; n < 12; ++n) {
    const int h = n / 3, t = n % 3;
    const int c = VS == 0 ? n : VS == 1 ? 3 * h + (t < 2 ? t ^ c1 : 2) : VS == 2 ? (n + 6 * c1) % 12 : (n + 3 * cls) % 12;
    s |= (unsigned long long)c << (4 * n);
  }
  return s;
}

DI void q_chunk(f32x4 (&acc)[6][4], v4u (&w)[3], v4u (&xf)[4], const unsigned (&xa)[4], const unsigned (&wa)[4], unsigned (&wc)[4],
                unsigned so, unsigned sn) {
  v4u xn[4];
  unsigned x0 = opq(xa[2] + so), x1 = opq(xa[3] + so);
#pragma unroll
  for (int i = 0; i < 12; ++i) {
    const int dt = i % 6;
    if (i == 6) {
#pragma unroll
      for (int q = 0; q < 4; ++q) xf[q] = xn[q];
    }
    if (i == 9) { x0 = opq(xa[0] + sn); x1 = opq(xa[1] + sn);
#pragma unroll
      for (int q = 0; q < 4; ++q) wc[q] = opq(wa[q] + sn);
    }
    if (i == 10) { bar(); w[0] = ldstr(wc[0]); SB(); }
    acc[dt][0] = mfma(w[i % 3], xf[0], acc[dt][0]);
    if (i < 4) xn[i] = lds128((i < 2 ? x0 : x1) + (i & 1) * 2048);
    if (i == 10) xn[0] = lds128(x0);
    SB();
    acc[dt][1] = mfma(w[i % 3], xf[1], acc[dt][1]);
    if (i == 10) xn[1] = lds128(x0 + 2048);
    SB();
    acc[dt][2] = mfma(w[i % 3], xf[2], acc[dt][2]);
    if (i == 10) xn[2] = lds128(x1);
    SB();
    acc[dt][3] = mfma(w[i % 3], xf[3], acc[dt][3]);
    if (i == 10) xn[3] = lds128(x1 + 2048);
    if (i < 9) { const int k = i + 3, d = k % 6; w[i % 3] = ldstr(wc[d & 3] + (d >> 2) * 8192 + (k / 6) * 4096); }
    if (i >= 10) w[i % 3] = ldstr(wc[i - 9]);
    SB();
  }
#pragma unroll
  for (int q = 0; q < 4; ++q) xf[q] = xn[q];
}
template <bool FIRST>
DI void v_chunk(f32x4 (&acc)[10][2], v4u (&w)[5], v4u (&vf)[2], const unsigned (&va)[2], const unsigned (&wa)[4], unsigned (&wc)[4],
                unsigned so, unsigned sn) {
  v4u vn[2];
  unsigned v0 = opq(va[1] + so);
#pragma unroll
  for (int i = 0; i < 20; ++i) {
    const int nt = i % 10;
    if (i == 10) { vf[0] = vn[0]; vf[1] = vn[1]; }
    if (i == 15) { v0 = opq(va[0] + sn);
#pragma unroll
      for (int q = 0; q < 4; ++q) wc[q] = opq(wa[q] + sn);
    }
    if (i == 17) { bar(); vn[0] = lds128(v0); w[0] = ldstr(wc[0]); SB(); }
    acc[nt][0] = mfma(vf[0], w[i % 5], FIRST && i < 10 ? (f32x4){} : acc[nt][0]);
    if (i < 2) vn[i] = lds128(v0 + i * 2048);
    if (i == 17) { vn[1] = lds128(v0 + 2048); w[1] = ldstr(wc[1]); }
    SB();
    acc[nt][1] = mfma(vf[1], w[i % 5], FIRST && i < 10 ? (f32x4){} : acc[nt][1]);
    if (i < 15) { const int k = i + 5, n = k % 10; w[i % 5] = ldstr(wc[n & 3] + (n >> 2) * 8192 + (k / 10) * 4096); }
    if (i >= 17) { const int n = i - 15; w[n] = ldstr(wc[n & 3] + (n >> 2) * 8192); }
    SB();
  }
  vf[0] = vn[0]; vf[1] = vn[1];
}
DI float xmax(float m) {
  auto a = __builtin_amdgcn_permlane16_swap(__float_as_uint(m), __float_as_uint(m), false, false);
  m = fmaxf(__uint_as_float(a[0]), __uint_as_float(a[1]));
  auto b = __builtin_amdgcn_permlane32_swap(__float_as_uint(m), __float_as_uint(m), false, false);
  return fmaxf(__uint_as_float(b[0]), __uint_as_float(b[1]));
}
DI float xsum(float m) {
  auto a = __builtin_amdgcn_permlane16_swap(__float_as_uint(m), __float_as_uint(m), false, false);
  m = __uint_as_float(a[0]) + __uint_as_float(a[1]);
  auto b = __builtin_amdgcn_permlane32_swap(__float_as_uint(m), __float_as_uint(m), false, false);
  return __uint_as_float(b[0]) + __uint_as_float(b[1]);
}
DI v4u softmax(f32x4 s0, f32x4 s1) {
  const float c = 0.07216878364870323f * 1.4426950408889634f;
  const float m = xmax(fmaxf(fmaxf(fmaxf(s0[0], s0[1]), fmaxf(s0[2], s0[3])), fmaxf(fmaxf(s1[0], s1[1]), fmaxf(s1[2], s1[3]))));
  const float mc = -m * c;
#pragma unroll
  for (int r = 0; r < 4; ++r) { s0[r] = __builtin_amdgcn_exp2f(fmaf(s0[r], c, mc)); s1[r] = __builtin_amdgcn_exp2f(fmaf(s1[r], c, mc)); }
  const float rl = __builtin_amdgcn_rcpf(xsum(((s0[0] + s0[1]) + (s0[2] + s0[3])) + ((s1[0] + s1[1]) + (s1[2] + s1[3]))));
  return pack2(s0 * rl, s1 * rl);
}

__global__ void __launch_bounds__(512, 2)
mha_block_kernel(const char* __restrict__ x, const char* __restrict__ kc, const char* __restrict__ vc,
                 const char* __restrict__ wq, const char* __restrict__ wo, char* __restrict__ out, char* ws) {
  __shared__ __attribute__((aligned(1024))) char smem[163840];
  asm volatile("" :: "s"(x), "s"(kc), "s"(vc), "s"(wq), "s"(wo), "s"(out), "s"(ws));
  const unsigned lb = (unsigned)(size_t)(LAS char*)smem;
  const int wv = __builtin_amdgcn_readfirstlane(threadIdx.x >> 6), lane = threadIdx.x & 63;
  const int bid = blockIdx.x, xi = bid & 7, j = (bid >> 3) & 3, rg = (bid >> 5) & 3;
  const bool isv = bid >= 128;
  const int b = wv & 3, cls = j ^ rg;
  ST_INIT(); ST(0);
  const auto rs = __builtin_amdgcn_make_buffer_rsrc(ws, 0, WS_END, 0x00020000);
  LAS unsigned* tagw = (LAS unsigned*)(lb + SPARE + 32768);
  const unsigned c = lane & 15, g = lane >> 4;
  unsigned tag;

  if (__builtin_expect(wv < 4, 1)) {
    const unsigned l8 = lane >> 3, p = lane & 7;
    const int c0 = cls & 1;
    const unsigned pa = isv ? 384 : 1280, pw = isv ? 1280 : 12288;
    const unsigned sw0 = (p ^ g) << 4, sw1 = sw0 ^ 64;
    const unsigned va0 = l8 * pa + sw0, va1 = (8 + l8) * pa + sw1;
    const unsigned vw0 = (l8 + 8 * c0) * pw + ((p ^ (((g & 1) | (c0 << 1)) << 1)) << 4);
    const unsigned vw1 = (l8 + 8 * (c0 ^ 1)) * pw + ((p ^ (((g & 1) | ((c0 ^ 1) << 1)) << 1)) << 4);
    const char* ab = isv ? vc + (size_t)((rg * 4 + b) * 32 + 4 * xi) * 12288 : x + (size_t)(rg * 128 + b * 32) * 1280;
    const char* wb = isv ? wo + (size_t)(xi * 768 + b * 16) * 1280 + ((j * 320) & ~127) : wq + (size_t)(b * 16) * 12288 + (4 * xi + j) * 384;
    const char *ab0 = ab + c0 * 16 * pa, *ab1 = ab + (c0 ^ 1) * 16 * pa;
    const unsigned la0 = lb + b * 4096 + c0 * 2048, la1 = la0 ^ 2048, lw0 = lb + WT + b * 2048 + c0 * 1024, lw1 = lw0 ^ 1024;
    unsigned long long sch = isv ? (cls == 0 ? sched_v(0, 0) : cls == 1 ? sched_v(0, 1) : cls == 2 ? sched_v(1, 2) : sched_v(1, 3))
                                 : (cls == 0 ? sched_q(0, 0) : cls == 1 ? sched_q(0, 1) : cls == 2 ? sched_q(1, 2) : sched_q(1, 3));
    const int nstep = isv ? 12 : 10;
    unsigned so = 0;
#pragma unroll 1
    for (int n = 0; n < nstep; ++n) {
      const unsigned ch = (unsigned)sch & 15u; sch >>= 4;
      const unsigned ao = ch * 128 + (isv ? ((ch * 11) >> 5) * (12288 - 384) : 0u);
      const char* w_ = wb + ch * 64 * pw;
      dma<0, 0>(va0, ab0 + ao, la0 + so); dma<1024, 0>(va1, ab0 + ao, la0 + so);
      dma<0, 0>(vw0, w_, lw0 + so); dma<8192, 128>(vw0, w_, lw0 + so); dma<16384, 256>(vw0, w_, lw0 + so);
      dma<0, 0>(va0, ab1 + ao, la1 + so); dma<1024, 0>(va1, ab1 + ao, la1 + so);
      dma<0, 0>(vw1, w_, lw1 + so); dma<8192, 128>(vw1, w_, lw1 + so); dma<16384, 256>(vw1, w_, lw1 + so);
      so = nxt(so);
      if (n) { wait_vm<10>(); bar(); ST(n); }
    }
    if (isv) {
      wait_vm<0>(); bar(); ST(12);
      bar();
      ST_DUMP();
      return;
    }
    { const unsigned k0 = l8 * 384 + sw0, k1 = (8 + l8) * 384 + sw1;
      const char* kb = kc + (size_t)((rg * 4 + b) * 32 + 4 * xi + j) * 12288;
      const unsigned l0 = lb + SPARE + b * 4096, l2 = lb + SLOT + b * 4096;
      dma<0, 0>(k0, kb, l0); dma<16384, 128>(k0, kb, l0); dma<0, 256>(k0, kb, l2);
      dma<1024, 0>(k1, kb, l0); dma<17408, 128>(k1, kb, l0); dma<1024, 256>(k1, kb, l2);
      kb += 6144;
      dma<2048, 0>(k0, kb, l0); dma<18432, 128>(k0, kb, l0); dma<2048, 256>(k0, kb, l2);
      dma<3072, 0>(k1, kb, l0); dma<19456, 128>(k1, kb, l0); dma<3072, 256>(k1, kb, l2); }
    wait_vm<12>(); bar(); ST(10);
    wait_vm<0>(); bar(); ST(11);
    tag = __builtin_amdgcn_readfirstlane(*tagw);
    bar();
  } else {
    tag = __builtin_amdgcn_readfirstlane(__hip_atomic_load((gu32*)(ws + WS_EP + rg * 256), RLX_AGENT)) + 1u;
    const unsigned q4 = c >> 2, p = lane & 3, sw = (q4 >> 1) | ((g & 1) << 1);
    unsigned wa[4], wc[4];
    const int t0 = isv ? 2 * (j & 1) : 6 * (b & 1);
#pragma unroll
    for (int i = 0; i < 4; ++i)
      wc[i] = wa[i] = opq(lb + WT + ((t0 + i) >> 2) * 8192 + (8 * g + q4) * 128 + ((((t0 + i) & 3) ^ sw) << 5) + 8 * p);
    unsigned so = 0;
    if (isv) {
      unsigned va[2];
#pragma unroll
      for (int s = 0; s < 2; ++s) va[s] = opq(lb + (b * 32 + c) * 128 + (((4 * s + g) ^ (c >> 1)) << 4));
      const unsigned stash = lb + SPARE + b * 10240 + lane * 16;
      v4u w[5], vf[2], h1[10], h2[10], h3[10];
      bar(); ST(1);
      vf[0] = lds128(va[0]); vf[1] = lds128(va[0] + 2048);
#pragma unroll
      for (int i = 0; i < 5; ++i) { w[i] = ldstr(wa[i & 3] + (i >> 2) * 8192); SB(); }
#pragma unroll
      for (int n = 0; n < 10; ++n) { asm volatile("" : "=v"(h1[n])); asm volatile("" : "=v"(h2[n])); }
      f32x4 acc[10][2];
#pragma unroll 1
      for (int hh = 0;; ++hh) {
        v_chunk<true>(acc, w, vf, va, wa, wc, 0, SLOT);
        so = SLOT;
#pragma unroll 1
        for (int n = 0; n < 2; ++n) { const unsigned sn = nxt(so); v_chunk<false>(acc, w, vf, va, wa, wc, so, sn); so = sn; }
        if (hh == 3) break;
        if (hh == 2) {
#pragma unroll
          for (int n = 0; n < 10; ++n) *(LAS v4u*)(stash + n * 1024) = h1[n];
        }
#pragma unroll
        for (int n = 0; n < 10; ++n) { h1[n] = h2[n]; h2[n] = pack2(acc[n][0], acc[n][1]); SB(); }
      }
#pragma unroll
      for (int n = 0; n < 10; ++n) { h3[n] = pack2(acc[n][0], acc[n][1]); asm volatile("" : "+v"(h3[n])); }
      SB();
      v4u h0[10];
#pragma unroll
      for (int n = 0; n < 10; ++n) h0[n] = lds128(stash + n * 1024);
      ST(13);
      const int hrot = VS == 2 ? (cls & 2) : VS == 3 ? cls : 0;
      const v4u tb = tag_bits(tag, lane), ts = tb << 15;
      v4u pf[4][2];
      for (int spins = 0; spins < kSpinCap; ++spins) {
        unsigned bad = 0;
#pragma unroll
        for (int e = 0; e < 4; ++e)
#pragma unroll
          for (int q = 0; q < 2; ++q)
            pf[e][q] = __builtin_amdgcn_raw_buffer_load_b128(rs, lane * 16 + q * 1024, WS_P + (((xi * 4 + rg) * 4 + ((e + hrot) & 3)) * 4 + b) * 2048, 16);
#pragma unroll
        for (int e = 0; e < 4; ++e)
#pragma unroll
          for (int q = 0; q < 2; ++q) { pf[e][q] ^= ts; bad |= (pf[e][q][0] | pf[e][q][1]) | (pf[e][q][2] | pf[e][q][3]); }
        if (__all((bad & 0x80008000u) == 0)) break;
        asm volatile("" ::: "memory");
      }
      ST(14);
      gu32* fl = (gu32*)(ws + WS_FO + (((rg * 4 + j) * 8 + xi) * 4 + b) * 128);
      const unsigned xo = WS_X + (((rg * 4 + j) * 8 + 2 * b) * 8 + xi) * 5120;
#pragma unroll
      for (int m = 0; m < 5; ++m) {
        if (XM == 1 && m == HINT) { if (lane == 0) __hip_atomic_store(fl, tag, RLX_AGENT); }
        f32x4 o[2][2];
#pragma unroll
        for (int q = 0; q < 2; ++q)
#pragma unroll
          for (int k = 0; k < 2; ++k) {
            o[q][k] = mfma(h0[2 * m + k], pf[0][q], (f32x4){});
            o[q][k] = mfma(h1[2 * m + k], pf[1][q], o[q][k]);
            o[q][k] = mfma(h2[2 * m + k], pf[2][q], o[q][k]);
            o[q][k] = mfma(h3[2 * m + k], pf[3][q], o[q][k]);
          }
#pragma unroll
        for (int q = 0; q < 2; ++q) {
          v4u d = pack2(o[q][0], o[q][1]);
          if (XM == 1) d = (d & 0xfffefffeu) | tb;
          __builtin_amdgcn_raw_buffer_store_b128(d, rs, lane * 16 + m * 1024, xo + q * (8 * 5120), 16);
        }
      }
      if (XM == 1 && HINT == 5) { if (lane == 0) __hip_atomic_store(fl, tag, RLX_AGENT); }
      ST(15);
      if (XM == 0) { wait_vm<0>(); if (lane == 0) __hip_atomic_store(fl, tag, RLX_AGENT); }
      ST(16);
      ST_DUMP();
      return;
    }
    if (wv == 4 && lane == 0) *tagw = tag;
    const int bp = b >> 1, dq = b & 1;
    unsigned xa[4];
#pragma unroll
    for (int i = 0; i < 4; ++i) xa[i] = opq(lb + (bp * 64 + ((i & 1) ^ dq ^ 1) * 32 + c) * 128 + (((4 * (i >> 1) + g) ^ (c >> 1)) << 4));
    f32x4 acc[6][4];
#pragma unroll
    for (int d = 0; d < 6; ++d)
#pragma unroll
      for (int q = 0; q < 4; ++q) { acc[d][q] = (f32x4){}; asm volatile("" : "+v"(acc[d][q])); }
    v4u w[3], xf[4];
    bar(); ST(1);
    w[0] = ldstr(wa[0]);
#pragma unroll
    for (int q = 0; q < 4; ++q) xf[q] = lds128(xa[q >> 1] + (q & 1) * 2048);
    SB();
    w[1] = ldstr(wa[1]); w[2] = ldstr(wa[2]);
    SB();
#pragma unroll 1
    for (int n = 0; n < 10; ++n) { const unsigned sn = nxt(so); q_chunk(acc, w, xf, xa, wa, wc, so, sn); so = sn; }
    ST(13);
    f32x4 s[2][2][2];
    const unsigned swp = lb + 2 * SLOT + lane * 16;
#pragma unroll
    for (int e = 0; e < 2; ++e) {
      v4u kf[3][2];
#pragma unroll
      for (int m = 0; m < 3; ++m) {
        const int M = 3 * dq + m;
        const unsigned ka = lb + (M >= 4 ? SLOT : SPARE + (M >> 1) * 16384) + (bp * 64 + (e ^ dq ^ 1) * 32 + c) * 128 +
                            (((4 * (M & 1) + (g >> 1)) ^ (c >> 1)) << 4) + 8 * (g & 1);
#pragma unroll
        for (int k = 0; k < 2; ++k) { const v2u lo = lds64(ka + k * 2048), hi = lds64((ka ^ 32) + k * 2048); kf[m][k] = (v4u){lo[0], lo[1], hi[0], hi[1]}; }
      }
#pragma unroll
      for (int m = 0; m < 3; ++m)
#pragma unroll
        for (int q = 0; q < 2; ++q) {
          const v4u qf = pack2(acc[2 * m][2 * e + q], acc[2 * m + 1][2 * e + q]);
#pragma unroll
          for (int k = 0; k < 2; ++k) s[e][q][k] = mfma(kf[m][k], qf, m ? s[e][q][k] : (f32x4){});
        }
      if (e == 0) {
#pragma unroll
        for (int i = 0; i < 4; ++i) *(LAS f32x4*)(swp + b * 4096 + i * 1024) = s[0][i >> 1][i & 1];
      }
    }
    bar();
    ST(14);
    const v4u ts = tag_bits(tag, lane) << 15;
#pragma unroll
    for (int q = 0; q < 2; ++q) {
      const f32x4 s0 = s[1][q][0] + *(const LAS f32x4*)(swp + (b ^ 1) * 4096 + q * 2048);
      const f32x4 s1 = s[1][q][1] + *(const LAS f32x4*)(swp + (b ^ 1) * 4096 + q * 2048 + 1024);
      const v4u pv = (softmax(s0, s1) & 0x7fff7fffu) | ts;
      __builtin_amdgcn_raw_buffer_store_b128(pv, rs, lane * 16 + q * 1024, WS_P + (((xi * 4 + rg) * 4 + j) * 4 + b) * 2048, 16);
    }
    ST(15);
  }
  v4u id;
  { const bool on = (c >> 3) == (g & 1); const unsigned one = (c & 1) ? 0x3f800000u : 0x00003f80u, j2 = (c & 7) >> 1;
    id = (v4u){on && j2 == 0 ? one : 0u, on && j2 == 1 ? one : 0u, on && j2 == 2 ? one : 0u, on && j2 == 3 ? one : 0u}; }
  v4u tb = tag_bits(tag, lane);
  asm volatile("" : "+v"(id), "+v"(tb));
  if (wv == 0) poll((gu32*)(ws + WS_FO + (((rg * 4 + j) * 8 + (lane & 7)) * 4 + (xi >> 1)) * 128), lane < 8, tag);
  bar();
  if (wv == 7 && xi == 0 && j == 0) {
    poll((gu32*)(ws + WS_FO + (rg * 32 + (lane & 31)) * 512), lane < 32, tag);
    if (lane == 0) __hip_atomic_store((gu32*)(ws + WS_EP + rg * 256), tag, RLX_AGENT);
  }
  if (wv < 5) {
    ST(17);
    const unsigned vo = (g >> 1) * 5120 + (lane & 31) * 16;
    const unsigned o = WS_X + ((rg * 4 + j) * 8 + xi) * (8 * 5120) + wv * 1024;
    v4u d[2][4];
    for (int spins = 0; spins < kSpinCap; ++spins) {
#pragma unroll
      for (int e = 0; e < 4; ++e)
#pragma unroll
        for (int h = 0; h < 2; ++h) d[h][e] = __builtin_amdgcn_raw_buffer_load_b128(rs, vo + h * 512, o + e * 10240, 16);
      if (XM == 0) break;
      unsigned bad = 0;
#pragma unroll
      for (int e = 0; e < 4; ++e)
#pragma unroll
        for (int h = 0; h < 2; ++h) { const v4u t = d[h][e] ^ tb; bad |= (t[0] | t[1]) | (t[2] | t[3]); }
      if (__all((bad & 0x00010001u) == 0)) break;
      asm volatile("" ::: "memory");
    }
    const unsigned row = rg * 128 + xi * 16 + c, col = j * 160 + wv * 32 + (g & 1) * 16 + (g >> 1) * 4;
#pragma unroll
    for (int h = 0; h < 2; ++h) {
      f32x4 sum = {};
#pragma unroll
      for (int e = 0; e < 4; ++e) sum = mfma(id, d[h][e], sum);
      *(GAS v2u*)(out + row * 1280 + (col + 8 * h) * 2) = (v2u){pk(sum[0], sum[1]), pk(sum[2], sum[3])};
    }
    ST(18);
  }
  ST_DUMP();
}

extern "C" void kernel_launch(void* const* d_in, const int* in_sizes, int n_in, void* d_out, int out_size,
                              void* d_ws, size_t ws_size, hipStream_t stream) {
  mha_block_kernel<<<256, 512, 0, stream>>>((const char*)d_in[0], (const char*)d_in[1], (const char*)d_in[2],
                                            (const char*)d_in[3], (const char*)d_in[4], (char*)d_out, (char*)d_ws);
}
```

```cpp
#define PCV 0x88800ull
#include <hip/hip_runtime.h>
#include <hip/hip_bf16.h>

typedef unsigned v4u __attribute__((ext_vector_type(4)));
typedef unsigned v2u __attribute__((ext_vector_type(2)));
typedef short s16x4 __attribute__((ext_vector_type(4)));
typedef short bf16x8 __attribute__((ext_vector_type(8)));
typedef __bf16 bf2 __attribute__((ext_vector_type(2)));
typedef float f32x2 __attribute__((ext_vector_type(2)));
typedef float f32x4 __attribute__((ext_vector_type(4)));
#define GAS __attribute__((address_space(1)))
#define LAS __attribute__((address_space(3)))
#define DI __device__ __forceinline__
typedef GAS unsigned gu32;
#define RLX_AGENT __ATOMIC_RELAXED, __HIP_MEMORY_SCOPE_AGENT
#define SB() __builtin_amdgcn_sched_barrier(0)

#define STAMP 0
#define QS 1
#define VS 4
#define RV 5
#define HQ 3
#define HV 2
#define DP 0
#define DR 0
#define KM 0
#define PQ 0.9
#define TUNE 0
#define BP 8
#define LW 3
#define DL 0
#define ND 0
#define NBQ 0
#define NBV NBQ
#define PCQ 0ull
#define CPF 1

constexpr int SLOT = 40960, WT = 16384, SPARE = 3 * SLOT;
constexpr int TAGW = SPARE + 32768, TSA = TAGW + 1024;
constexpr unsigned SLAB = 5376;
constexpr unsigned WS_EP = 0, WS_H = 4096, WS_P = WS_H + (128 + 1024) * 128, WS_X = WS_P + 512 * 2048,
                   WS_END = WS_X + 128 * 8 * SLAB, WS_ST = 32u << 20;
constexpr int kSpinCap = 1 << 22;

DI f32x4 mfma(v4u a, v4u b, f32x4 c) {
  return __builtin_amdgcn_mfma_f32_16x16x32_bf16(__builtin_bit_cast(bf16x8, a), __builtin_bit_cast(bf16x8, b), c, 0, 0, 0);
}
DI unsigned pk(float a, float b) { f32x2 v = {a, b}; return __builtin_bit_cast(unsigned, __builtin_convertvector(v, bf2)); }
DI v4u pack2(const f32x4& x, const f32x4& y) { return (v4u){pk(x[0], x[1]), pk(x[2], x[3]), pk(y[0], y[1]), pk(y[2], y[3])}; }
DI v4u lds128(unsigned a) { return *(const LAS v4u*)a; }
DI v2u lds64(unsigned a) { return *(const LAS v2u*)a; }
DI v2u ldtr(unsigned a) { return __builtin_bit_cast(v2u, __builtin_amdgcn_ds_read_tr16_b64_v4i16((LAS s16x4*)a)); }
struct Frag { v2u lo, hi; DI v4u v() const { return (v4u){lo[0], lo[1], hi[0], hi[1]}; } };
DI v4u ldstr(unsigned a) {
  s16x4 lo = __builtin_amdgcn_ds_read_tr16_b64_v4i16((LAS s16x4*)a);
  s16x4 hi = __builtin_amdgcn_ds_read_tr16_b64_v4i16((LAS s16x4*)(a + 512));
  return __builtin_bit_cast(v4u, __builtin_shufflevector(lo, hi, 0, 1, 2, 3, 4, 5, 6, 7));
}
template <int LOFF, int GOFF> DI void dma(unsigned voff, const char* sbase, unsigned lbase) {
  asm volatile("s_add_u32 m0, %2, %3\n\ts_nop 0\n\tglobal_load_lds_dwordx4 %0, %1 offset:%4"
               :: "v"(voff), "s"(sbase), "s"(lbase), "i"(LOFF - GOFF), "i"(GOFF) : "memory", "scc");
}
template <int N> DI void wait_vm() { asm volatile("s_waitcnt vmcnt(%0)" :: "i"(N) : "memory"); }
DI void bar() { asm volatile("s_waitcnt lgkmcnt(0)\n\ts_barrier" ::: "memory"); }
DI unsigned opq(unsigned x) { asm volatile("" : "+v"(x)); return x; }
DI unsigned nxt(unsigned so) { return so == 2 * SLOT ? 0u : so + SLOT; }
DI void poll(gu32* p, unsigned tag) {
#pragma unroll 1
  for (int spins = 0; spins < kSpinCap; ++spins)
    if (__all(__hip_atomic_load(p, RLX_AGENT) == tag)) break;
  __builtin_amdgcn_fence(__ATOMIC_ACQUIRE, "wavefront");
}
template <int N> DI void nap() { if (N > 0) __builtin_amdgcn_s_sleep(N); }
DI v4u tag_bits(unsigned epoch, int lane) {
  v4u m;
#pragma unroll
  for (int h = 0; h < 2; ++h) {
    const unsigned e = ((epoch >> (3 + 3 * h + 6 * (lane & 3))) + epoch) & 7u;
    m[2 * h] = (~e & 1u) | ((e & 1u) << 16);
    m[2 * h + 1] = ((e >> 1) & 1u) | ((e >> 2) << 16);
  }
  return m;
}
#define ST(n)
#define ST_INIT()
#define ST_DUMP()
#define ST_PAR
#define ST_ARG

constexpr unsigned long long sched_q(int c1, int cls) {
  unsigned long long s = 0;
  for (int n = 0; n < 10; ++n) {
    const int c = QS == 0 ? n : QS == 1 ? n ^ c1 : QS == 2 ? (n + 5 * c1) % 10 : QS == 3 ? (n + (5 * cls) / 2) % 10
                : (n == 0 || n == 9 ? n : ((n - 1) ^ c1) + 1);
    s |= (unsigned long long)c << (4 * n);
  }
  return s;
}
constexpr unsigned long long sched_v(int c1, int cls) {
  unsigned long long s = 0;
  for (int n = 0; n < 12; ++n) {
    const int h = n / 3, t = n % 3;
    const int c = VS == 0 ? n : VS == 1 ? 3 * h + (t < 2 ? t ^ c1 : 2) : VS == 2 ? (n + 6 * c1) % 12 : VS == 3 ? (n + 3 * cls) % 12
                : VS == 4 ? 3 * (h ^ c1) + t : VS == 5 ? 3 * h + (t > 0 ? ((t - 1) ^ c1) + 1 : 0) : (h < 2 ? n : 3 * (h ^ c1) + t);
    s |= (unsigned long long)c << (4 * n);
  }
  return s;
}
DI int head_of(int e, int cls) { return VS == 2 ? (e + (cls & 2)) & 3 : VS == 3 ? (e + cls) & 3 : VS == 4 || (VS == 6 && e >= 2) ? e ^ (cls >> 1) : e; }

DI unsigned wq_addr(const unsigned (&wc)[4], int k) { const int d = k % 6; return wc[d & 3] + (d >> 2) * 8192 + (k / 6) * 4096; }
DI void q_chunk(f32x4 (&acc)[6][4], Frag (&w)[4], v4u (&xf)[4], const unsigned (&xa)[4], const unsigned (&wa)[4], unsigned (&wc)[4],
                unsigned so, unsigned sn) {
  v4u xn[4];
  unsigned x0, x1;
#pragma unroll
  for (int i = 0; i < 12; ++i) {
    const int dt = i % 6;
    if (i == 6) {
#pragma unroll
      for (int q = 0; q < 4; ++q) xf[q] = xn[q];
    }
    if (i == 10) { bar(); w[0].lo = ldtr(wc[0]); SB(); }
    const v4u a = w[i & 3].v();
    acc[dt][0] = mfma(a, xf[0], acc[dt][0]); SB();
    if (i == 0) { x0 = opq(xa[2] + so); x1 = opq(xa[3] + so); }
    if (i >= 1 && i < 5) xn[i - 1] = lds128((i < 3 ? x0 : x1) + ((i - 1) & 1) * 2048);
    if (i == 9) { x0 = opq(xa[0] + sn); x1 = opq(xa[1] + sn); }
    if (i == 10) { w[0].hi = ldtr(wc[0] + 512); xn[0] = lds128(x0); }
    if (i == 11) w[1].lo = ldtr(wc[1]);
    SB();
    acc[dt][1] = mfma(a, xf[1], acc[dt][1]); SB();
    if (i < 9) w[(i + 3) & 3].lo = ldtr(wq_addr(wc, i + 3));
    if (i == 9) { wc[0] = opq(wa[0] + sn); wc[1] = opq(wa[1] + sn); }
    if (i == 10) xn[1] = lds128(x0 + 2048);
    if (i == 11) w[1].hi = ldtr(wc[1] + 512);
    SB();
    acc[dt][2] = mfma(a, xf[2], acc[dt][2]); SB();
    if (i < 9) w[(i + 3) & 3].hi = ldtr(wq_addr(wc, i + 3) + 512);
    if (i == 9) { wc[2] = opq(wa[2] + sn); wc[3] = opq(wa[3] + sn); }
    if (i == 10) xn[2] = lds128(x1);
    if (i == 11) w[2].lo = ldtr(wc[2]);
    SB();
    acc[dt][3] = mfma(a, xf[3], acc[dt][3]); SB();
    if (i == 10) xn[3] = lds128(x1 + 2048);
    if (i == 11) w[2].hi = ldtr(wc[2] + 512);
    SB();
  }
#pragma unroll
  for (int q = 0; q < 4; ++q) xf[q] = xn[q];
}
DI unsigned wo_addr(const unsigned (&wc)[4], int k) { const int n = k % 10; return wc[n & 3] + (n >> 2) * 8192 + (k / 10) * 4096; }
template <int T, bool CVT, bool NEXT>
DI void v_chunk(f32x4 (&acc)[10][2], v4u (&hold)[10], Frag (&w)[RV], v4u (&vf)[2], unsigned (&va)[2], unsigned (&wc)[4]) {
  constexpr int D = RV - 1, IB = 20 - D + 1;
  constexpr unsigned step = T == 2 ? -2 * SLOT : SLOT;
  v4u vn[2];
#pragma unroll
  for (int i = 0; i < 20; ++i) {
    const int nt = i % 10, r = (i + D) % RV;
    if (i == 10) { vf[0] = vn[0]; vf[1] = vn[1]; }
    if (NEXT && i == IB) { bar(); w[0].lo = ldtr(wc[0]); w[0].hi = ldtr(wc[0] + 512); vn[0] = lds128(va[0]); SB(); }
    if (CVT && i == 0) { hold[0] = pack2(acc[0][0], acc[0][1]); asm volatile("" : "+v"(hold[0])); SB(); }
    const v4u bw = w[i % RV].v();
    acc[nt][0] = mfma(vf[0], bw, T == 0 && i < 10 ? (f32x4){} : acc[nt][0]); SB();
    if (i + D < 20) w[r].lo = ldtr(wo_addr(wc, i + D));
    if (i == 4 || i == 5) vn[i - 4] = lds128(va[1] + (i - 4) * 2048);
    if (NEXT && i + D == 20) { wc[0] = opq(wc[0] + step); wc[1] = opq(wc[1] + step); va[0] = opq(va[0] + step); }
    if (NEXT && i >= IB) w[r].lo = ldtr(wo_addr(wc, i + D - 20));
    if (NEXT && i == IB) vn[1] = lds128(va[0] + 2048);
    if (CVT && i < 9) { hold[i + 1][0] = pk(acc[i + 1][0][0], acc[i + 1][0][1]); hold[i + 1][1] = pk(acc[i + 1][0][2], acc[i + 1][0][3]); }
    if (!NEXT && i >= 12) { hold[i - 12][0] = pk(acc[i - 12][0][0], acc[i - 12][0][1]); hold[i - 12][1] = pk(acc[i - 12][0][2], acc[i - 12][0][3]); }
    SB();
    acc[nt][1] = mfma(vf[1], bw, T == 0 && i < 10 ? (f32x4){} : acc[nt][1]); SB();
    if (i + D < 20) w[r].hi = ldtr(wo_addr(wc, i + D) + 512);
    if (NEXT && i + D == 20) { wc[2] = opq(wc[2] + step); wc[3] = opq(wc[3] + step); va[1] = opq(va[1] + step); }
    if (NEXT && i >= IB) w[r].hi = ldtr(wo_addr(wc, i + D - 20) + 512);
    if (CVT && i < 9) { hold[i + 1][2] = pk(acc[i + 1][1][0], acc[i + 1][1][1]); hold[i + 1][3] = pk(acc[i + 1][1][2], acc[i + 1][1][3]);
                        asm volatile("" : "+v"(hold[i + 1])); }
    if (!NEXT && i >= 12) { hold[i - 12][2] = pk(acc[i - 12][1][0], acc[i - 12][1][1]); hold[i - 12][3] = pk(acc[i - 12][1][2], acc[i - 12][1][3]);
                            asm volatile("" : "+v"(hold[i - 12])); }
    SB();
  }
  if (NEXT) { vf[0] = vn[0]; vf[1] = vn[1]; }
}
DI float vmax(float a, float b) { float r; asm("v_max_f32 %0, %1, %2" : "=v"(r) : "v"(a), "v"(b)); return r; }
DI float vmax3(float a, float b, float c) { float r; asm("v_max3_f32 %0, %1, %2, %3" : "=v"(r) : "v"(a), "v"(b), "v"(c)); return r; }
template <bool MAX> DI void xred(float& a, float& b) {
  if (MAX) asm volatile("s_nop 1" : "+v"(a), "+v"(b));
  auto r = __builtin_amdgcn_permlane16_swap(__float_as_uint(a), __float_as_uint(b), false, false);
  float y = MAX ? vmax(__uint_as_float(r[0]), __uint_as_float(r[1])) : __uint_as_float(r[0]) + __uint_as_float(r[1]);
  r = __builtin_amdgcn_permlane32_swap(__float_as_uint(y), __float_as_uint(y), false, false);
  y = MAX ? vmax(__uint_as_float(r[0]), __uint_as_float(r[1])) : __uint_as_float(r[0]) + __uint_as_float(r[1]);
  y = MAX ? y * -(0.07216878364870323f * 1.4426950408889634f) : __builtin_amdgcn_rcpf(y);
  r = __builtin_amdgcn_permlane16_swap(__float_as_uint(y), __float_as_uint(y), false, false);
  a = __uint_as_float(r[0]); b = __uint_as_float(r[1]);
}

typedef unsigned long long u64;
typedef u64 u64x4 __attribute__((ext_vector_type(4)));
typedef u64 u64x2 __attribute__((ext_vector_type(2)));
struct Args {
  u64x4 a; u64x2 b; u64 c;
  DI void load() { asm volatile("s_load_dwordx8 %0, %3, 0x0\n\ts_load_dwordx4 %1, %3, 0x20\n\ts_load_dwordx2 %2, %3, 0x30"
                                : "=&s"(a), "=&s"(b), "=&s"(c) : "s"(__builtin_amdgcn_kernarg_segment_ptr())); }
  DI void wait() { asm volatile("s_waitcnt lgkmcnt(0)" : "+s"(a), "+s"(b), "+s"(c)); }
};
DI void warm_up(v4u (&a)[3], u64 code, unsigned nd, int wv, unsigned tid) {
  unsigned s0, s1, s2, s3;
  asm volatile("s_cmp_lt_u32 %9, 4\n\ts_cbranch_scc1 9f\n\t"
               "s_load_dword %3, %7, 0x600\n\ts_load_dword %4, %7, 0x2600\n\ts_load_dword %5, %7, 0x4600\n\t"
               "v_mul_u32_u24 v2, 0x9e3779, %8\n\t"
               "v_mul_u32_u24 v3, 0x85ebcb, v2\n\t"
               "v_mul_u32_u24 v4, 0xc2b2af, v3\n\t"
               "v_mul_u32_u24 v5, 0x27d4eb, v4\n\t"
               "v_mul_u32_u24 v6, 0x165667, v5\n\t"
               "v_mul_u32_u24 v7, 0xd3a265, v6\n\t"
               "v_mul_u32_u24 v8, 0xfd7047, v7\n\t"
               "v_mul_u32_u24 v9, 0xb55a4f, v8\n\t"
               "v_mul_u32_u24 v10, 0x7f4a7d, v9\n\t"
               "v_mul_u32_u24 v11, 0x3c6ef3, v10\n\t"
               "v_mul_u32_u24 v12, 0xa54ff5, v11\n\t"
               "v_mul_u32_u24 v13, 0x510e53, v12\n\t"
               "v_mov_b32 v14, 0x3e003e00\n\t"
               "s_mov_b32 %6, 0x81ff81ff\n\t"
               "v_and_or_b32 v2, v2, %6, v14\n\t"
               "v_and_or_b32 v3, v3, %6, v14\n\t"
               "v_and_or_b32 v4, v4, %6, v14\n\t"
               "v_and_or_b32 v5, v5, %6, v14\n\t"
               "v_and_or_b32 v6, v6, %6, v14\n\t"
               "v_and_or_b32 v7, v7, %6, v14\n\t"
               "v_and_or_b32 v8, v8, %6, v14\n\t"
               "v_and_or_b32 v9, v9, %6, v14\n\t"
               "v_and_or_b32 v10, v10, %6, v14\n\t"
               "v_and_or_b32 v11, v11, %6, v14\n\t"
               "v_and_or_b32 v12, v12, %6, v14\n\t"
               "v_and_or_b32 v13, v13, %6, v14\n\t"
               "s_mov_b32 %6, %10\n\t"
               "s_nop 0\n\t"
               "1:\n\ts_sub_u32 %6, %6, 1\n\ts_cbranch_scc1 3f\n\t"
               "v_mfma_f32_16x16x32_bf16 v[14:17], v[2:5], v[4:7], 0\n\t"
               "v_mfma_f32_16x16x32_bf16 v[18:21], v[2:5], v[10:13], 0\n\t"
               "v_mfma_f32_16x16x32_bf16 v[22:25], v[2:5], v[6:9], 0\n\t"
               "v_mfma_f32_16x16x32_bf16 v[26:29], v[2:5], v[2:5], 0\n\t"
               "v_mfma_f32_16x16x32_bf16 v[14:17], v[4:7], v[8:11], 0\n\t"
               "v_mfma_f32_16x16x32_bf16 v[18:21], v[4:7], v[4:7], 0\n\t"
               "v_mfma_f32_16x16x32_bf16 v[22:25], v[4:7], v[10:13], 0\n\t"
               "v_mfma_f32_16x16x32_bf16 v[26:29], v[4:7], v[6:9], 0\n\t"
               "v_mfma_f32_16x16x32_bf16 v[14:17], v[6:9], v[2:5], 0\n\t"
               "v_mfma_f32_16x16x32_bf16 v[18:21], v[6:9], v[8:11], 0\n\t"
               "v_mfma_f32_16x16x32_bf16 v[22:25], v[6:9], v[4:7], 0\n\t"
               "v_mfma_f32_16x16x32_bf16 v[26:29], v[6:9], v[10:13], 0\n\t"
               "v_mfma_f32_16x16x32_bf16 v[14:17], v[8:11], v[6:9], 0\n\t"
               "v_mfma_f32_16x16x32_bf16 v[18:21], v[8:11], v[2:5], 0\n\t"
               "v_mfma_f32_16x16x32_bf16 v[22:25], v[8:11], v[8:11], 0\n\t"
               "v_mfma_f32_16x16x32_bf16 v[26:29], v[8:11], v[4:7], 0\n\t"
               "s_branch 1b\n"
               "3:\n\ts_nop 7\n\ts_waitcnt lgkmcnt(0)\n"
               "9:"
               : "=&{v[2:5]}"(a[0]), "=&{v[6:9]}"(a[1]), "=&{v[10:13]}"(a[2]), "=&s"(s0), "=&s"(s1), "=&s"(s2), "=&s"(s3)
               : "s"(code), "v"(tid), "s"(wv), "s"(nd) : "scc", "v14", "v15", "v16", "v17", "v18", "v19", "v20", "v21", "v22", "v23", "v24", "v25", "v26", "v27", "v28", "v29");
}
template <int N> DI void warm_more(const v4u (&a)[3]) {
  f32x4 t[4];
#pragma unroll
  for (int i = 0; i < N; ++i) t[i & 3] = mfma(a[(i / 3) % 3], a[i % 3], i < 4 ? (f32x4){} : t[i & 3]);
  if (N) asm volatile("" :: "v"(t[0]), "v"(t[1]), "v"(t[2]), "v"(t[3]));
#pragma unroll
  for (int i = 0; i < N; ++i) { __builtin_amdgcn_sched_group_barrier(0x8, 1, 0); __builtin_amdgcn_sched_group_barrier(0x2, 2, 0); }
}
DI unsigned sopq(unsigned x) { asm volatile("" : "+s"(x)); return x; }
template <bool V>
DI unsigned stream(Args& A, unsigned lb, int lane, int b, int xi, int j, int rg ST_PAR) {
  constexpr unsigned pa = V ? 384 : 1280, pw = V ? 1280 : 12288;
  if (!TUNE) { A.load(); SB(); }
  const unsigned l8 = lane >> 3, p = lane & 7, g = lane >> 4;
  const int cls = j ^ rg, c0 = cls & 1;
  const unsigned sw0 = (p ^ g) << 4;
  const unsigned va0 = opq(l8 * pa + sw0), va1 = opq((va0 ^ 64) + 8 * pa);
  const unsigned vw0 = opq(l8 * pw + ((p ^ (((g & 1) | (c0 << 1)) << 1)) << 4)), vw1 = opq(vw0 ^ 64);
  const unsigned a0 = V ? ((rg * 4 + b) * 32 + 4 * xi) * 12288 : (rg * 128 + b * 32) * 1280;
  const unsigned w0 = V ? (xi * 768 + b * 16) * 1280 + ((j * 320) & ~127) : b * 16 * 12288 + (4 * xi + j) * 384;
  const unsigned ao0 = sopq(a0 + c0 * 16 * pa), ao1 = sopq(a0 + (c0 ^ 1) * 16 * pa), wo0 = sopq(w0 + c0 * 8 * pw), wo1 = sopq(w0 + (c0 ^ 1) * 8 * pw);
  const unsigned la0 = sopq(lb + b * 4096 + c0 * 2048), la1 = la0 ^ 2048, lw0 = sopq(lb + WT + b * 2048 + c0 * 1024), lw1 = lw0 ^ 1024;
  constexpr unsigned long long s0 = V ? sched_v(0, 0) : sched_q(0, 0), s1 = V ? sched_v(0, 1) : sched_q(0, 1),
                               s2 = V ? sched_v(1, 2) : sched_q(1, 2), s3 = V ? sched_v(1, 3) : sched_q(1, 3);
  unsigned long long sch = (cls & 2) ? (c0 ? s3 : s2) : (c0 ? s1 : s0);
  unsigned km = KM >> 1;
  u64 pace = (V ? PCV : PCQ) >> 4;
  asm volatile("" : "+s"(sch));
  if (TUNE) A.load();
  A.wait();
  const char *act = (const char*)(V ? A.a[2] : A.a[0]), *wt = (const char*)(V ? A.b[0] : A.a[3]), *kc = (const char*)A.a[1], *ws = (const char*)A.c;
  unsigned so = 0, ep;
  auto chunk = [&](int at, int n) {
    unsigned ch = (unsigned)sch & 15u; sch >>= 4;
    if (TUNE) ch = ch < (V ? 12u : 10u) ? ch : 0u;
    const unsigned ao = ch * 128 + (V ? ((ch * 11) >> 5) * (12288 - 384) : 0u), wo = ch * (64 * pw);
    auto publish = [&](int i) { if ((i == BP || (!V && i == BP - 2)) && at == i) { wait_vm<BP>(); asm volatile("s_barrier" ::: "memory"); ST(n); } };
    const char *pa0 = act + (ao0 + ao), *pw0 = wt + (wo0 + wo);
    dma<0, 0>(va0, pa0, la0 + so); dma<1024, 0>(va1, pa0, la0 + so);
    dma<0, 0>(vw0, pw0, lw0 + so); dma<8192, 128>(vw0, pw0, lw0 + so); publish(4); dma<16384, 256>(vw0, pw0, lw0 + so); publish(5);
    const char *pa1 = act + (ao1 + ao), *pw1 = wt + (wo1 + wo);
    dma<0, 0>(va0, pa1, la1 + so); publish(6); dma<1024, 0>(va1, pa1, la1 + so); publish(7);
    dma<0, 0>(vw1, pw1, lw1 + so); publish(8); dma<8192, 128>(vw1, pw1, lw1 + so); publish(9); dma<16384, 256>(vw1, pw1, lw1 + so); publish(10);
    so = nxt(so);
  };
  chunk(-1, 0);
  asm volatile("global_load_dword %0, %1, %2 sc1" : "=v"(ep) : "v"(0u), "s"(ws + WS_EP + rg * 256) : "memory");
  unsigned vk = l8 * 384 + sw0, lk = lb + SPARE + b * 4096, nk = 0;
  const char* kb = kc + (size_t)((rg * 4 + b) * 32 + 4 * xi + j) * 12288;
  auto kpair = [&]() { dma<0, 0>(vk, kb, lk); dma<16384, 128>(vk, kb, lk); vk = (vk ^ 64) + 3072; lk += 1024; ++nk; };
#pragma unroll 1
  for (int n = 1; n < (V ? 12 : 10); ++n) {
    const bool kstep = !V && (km & 1u) && (!TUNE || nk < 4); km >>= 1;
    if (TUNE || (V ? PCV : PCQ)) { for (unsigned s = (unsigned)pace & 15u; s; --s) __builtin_amdgcn_s_sleep(2); pace >>= 4; }
    if (kstep) kpair();
    chunk(kstep ? BP - 2 : BP, n);
  }
  if (!V) {
    const unsigned k0 = l8 * 384 + sw0, k1 = (k0 ^ 64) + 3072, l2 = lb + SLOT + b * 4096;
    dma<0, 256>(k0, kb, l2); dma<1024, 256>(k1, kb, l2); dma<2048, 256>(k0, kb + 6144, l2); dma<3072, 256>(k1, kb + 6144, l2);
    asm volatile("" : "+v"(ep));
    const unsigned tag = __builtin_amdgcn_readfirstlane(ep) + 1u;
    if (b == 0) { *(LAS v4u*)(lb + TSA + lane * 16) = tag_bits(tag, lane) << 15; if (lane == 0) *(LAS unsigned*)(lb + TAGW) = tag; }
    if (nk <= 2) { kpair(); kpair(); wait_vm<8>(); } else wait_vm<4>();
    bar(); ST(10);
    while (nk < 4) kpair();
    return tag;
  }
  asm volatile("" : "+v"(ep));
  const unsigned tag = __builtin_amdgcn_readfirstlane(ep) + 1u;
  if (b == 0) { *(LAS v4u*)(lb + TSA + lane * 16) = tag_bits(tag, lane) << 15; if (lane == 0) *(LAS unsigned*)(lb + TAGW) = tag; }
  return tag;
}

__global__ void __launch_bounds__(512, 2)
mha_block_kernel(const char*, const char*, const char*, const char*, const char*, char*, char*) {
  __shared__ __attribute__((aligned(1024))) char smem[163840];
  Args A;
  unsigned long long pc;
  asm volatile("s_getpc_b64 %0" : "=s"(pc));
  const unsigned lb = (unsigned)(size_t)(LAS char*)smem;
  const int wv = __builtin_amdgcn_readfirstlane(threadIdx.x >> 6), lane = threadIdx.x & 63;
  const int bid = blockIdx.x, xi = bid & 7, j = (bid >> 3) & 3, rg = (bid >> 5) & 3;
  const bool isv = bid >= 128;
  const int b = wv & 3, cls = j ^ rg;
  ST_INIT(); ST(0);
#define ws ((char*)A.c)
#define out ((char*)A.b[1])
#define rs __builtin_amdgcn_make_buffer_rsrc(ws, 0, WS_END, 0x00020000)
#define hint ((gu32*)(ws + WS_H))
  LAS unsigned* tagw = (LAS unsigned*)(lb + TAGW);
  const unsigned tsa = lb + TSA;
  unsigned tag;

  v4u junk[3];
  {
    unsigned nd = ND;
    warm_up(junk, (pc & ~255ull) + ((bid >> 3) * 4 + b) * 64, nd, wv, threadIdx.x);
  }
  if (__builtin_expect(wv < 4, 1)) {
    if (__builtin_expect_with_probability(isv, 0, PQ)) tag = stream<true>(A, lb, lane, b, xi, j, rg ST_ARG);
    else tag = stream<false>(A, lb, lane, b, xi, j, rg ST_ARG);
    if (isv) {
      wait_vm<0>(); bar(); ST(12);
      poll(hint + (rg * 32 + b * 8 + xi) * 32, tag);
      ST(13);
      nap<DP>();
      bar();
      if (wv == 0 && xi == 0 && j == 0) {
        poll(hint + (128 + (rg * 32 + (lane & 31)) * 8) * 32, tag);
        if (lane == 0) __hip_atomic_store((gu32*)(ws + WS_EP + rg * 256), tag, RLX_AGENT);
      }
      ST_DUMP();
      __builtin_amdgcn_endpgm();
    }
    wait_vm<0>(); bar(); ST(11);
    bar();
  } else {
    A.load(); A.wait();
    const unsigned c = lane & 15, g = lane >> 4;
    const unsigned q4 = c >> 2, p = lane & 3, sw = (q4 >> 1) | ((g & 1) << 1);
    unsigned wc[4];
    const int t0 = isv ? 2 * (j & 1) : 6 * (b & 1);
#pragma unroll
    for (int i = 0; i < 4; ++i)
      wc[i] = opq(lb + WT + ((t0 + i) >> 2) * 8192 + (8 * g + q4) * 128 + ((((t0 + i) & 3) ^ sw) << 5) + 8 * p);
    if (isv) {
      unsigned va[2];
#pragma unroll
      for (int s = 0; s < 2; ++s) va[s] = opq(lb + (b * 32 + c) * 128 + (((4 * s + g) ^ (c >> 1)) << 4));
      Frag w[RV];
      v4u vf[2], h[4][10];
      f32x4 acc[10][2];
      warm_more<NBV>(junk);
      ST(20);
      bar(); ST(1);
      vf[0] = lds128(va[0]); vf[1] = lds128(va[0] + 2048);
#pragma unroll
      for (int i = 0; i < RV - 1; ++i) { w[i].lo = ldtr(wc[i]); w[i].hi = ldtr(wc[i] + 512); SB(); }
      v_chunk<0, false, true>(acc, h[0], w, vf, va, wc); v_chunk<1, false, true>(acc, h[0], w, vf, va, wc); v_chunk<2, false, true>(acc, h[0], w, vf, va, wc);
      v_chunk<0, true, true>(acc, h[0], w, vf, va, wc); v_chunk<1, false, true>(acc, h[0], w, vf, va, wc); v_chunk<2, false, true>(acc, h[0], w, vf, va, wc);
      v_chunk<0, true, true>(acc, h[1], w, vf, va, wc); v_chunk<1, false, true>(acc, h[0], w, vf, va, wc); v_chunk<2, false, true>(acc, h[0], w, vf, va, wc);
      v_chunk<0, true, true>(acc, h[2], w, vf, va, wc); v_chunk<1, false, true>(acc, h[0], w, vf, va, wc); v_chunk<2, false, false>(acc, h[3], w, vf, va, wc);
      const unsigned ln = opq(__builtin_amdgcn_mbcnt_hi(~0u, __builtin_amdgcn_mbcnt_lo(~0u, 0u)));
      const unsigned l16 = opq(ln * 16), l4 = opq(ln * 4);
      unsigned po[4];
#pragma unroll
      for (int e = 0; e < 4; ++e) {
          const int he = head_of(e, cls);
          po[e] = WS_P + ((xi * 4 + rg) * 16 + he * 4 + b) * 2048; asm volatile("" : "+s"(po[e])); }
      unsigned xo = WS_X + (((rg * 4 + j) * 8 + 2 * b) * 8 + xi) * SLAB;
      unsigned ho = WS_H + (128 + ((bid - 128) * 4 + b) * 2) * 128;
      asm volatile("" : "+s"(xo), "+s"(ho));
      const unsigned l0 = opq(ln ? WS_END : 0u);
      ST(13);
      bar();
      ST(14);
      v4u pf[2][4];
#pragma unroll
      for (int q = 0; q < 2; ++q)
#pragma unroll
        for (int e = 0; e < 4; ++e) { pf[q][e] = __builtin_amdgcn_raw_buffer_load_b128(rs, l16 + q * 1024, po[e], 16); SB(); }
      const v4u ts = lds128(tsa + l16);
      tag = __builtin_amdgcn_readfirstlane(*tagw);
#pragma unroll
      for (int n = 8; n < 10; ++n) { h[3][n] = pack2(acc[n][0], acc[n][1]); asm volatile("" : "+v"(h[3][n])); }
      SB();
      unsigned bad = 0, chk = tag;
#pragma unroll
      for (int e = 0; e < 4; ++e) { pf[0][e] ^= ts; bad |= (pf[0][e][0] | pf[0][e][1]) | (pf[0][e][2] | pf[0][e][3]); }
      f32x4 o[2][2];
      v4u d;
#pragma unroll
      for (int u = 0; u <= 10; ++u) {
        const int q = u / 5, m = u % 5, qp = (u - 1) / 5, mp = (u + 4) % 5;
        if (u < 10 && m == 0) {
          if (__builtin_expect(!__all((bad & 0x80008000u) == 0), 0)) {
#pragma unroll 1
            for (int spins = 0; spins < kSpinCap; ++spins) {
              nap<2>();
              bad = 0;
#pragma unroll
              for (int e = 0; e < 4; ++e) pf[q][e] = __builtin_amdgcn_raw_buffer_load_b128(rs, l16 + q * 1024, po[e], 16);
#pragma unroll
              for (int e = 0; e < 4; ++e) { pf[q][e] ^= ts; bad |= (pf[q][e][0] | pf[q][e][1]) | (pf[q][e][2] | pf[q][e][3]); }
              ST(19);
              if (__all((bad & 0x80008000u) == 0)) break;
            }
          }
          bad = 0;
          if (u == 0) ST(15);
        }
        if (u == HV) { __builtin_amdgcn_raw_buffer_store_b32(tag, rs, l0, ho, 16); SB(); }
#pragma unroll
        for (int i = 0; i < 8; ++i) {
          const int e = i >> 1, k = i & 1;
          if (u < 10) { o[u & 1][k] = mfma(h[e][2 * m + k], pf[q][e], e ? o[u & 1][k] : (f32x4){}); SB(); }
          if (u >= 1 && u <= 4) {
            v4u& t = pf[1][u - 1];
            if (i < 2) { t[2 * i] ^= ts[2 * i]; t[2 * i + 1] ^= ts[2 * i + 1]; }
            if (i == 2) bad = opq((bad | t[0] | t[1]) | t[2] | t[3]);
          }
          if (u > 0) {
            const f32x4(&s)[2] = o[(u - 1) & 1];
            if (i == 3 || i == 4) { d[2 * (i - 3)] = pk(s[i - 3][0], s[i - 3][1]); d[2 * (i - 3) + 1] = pk(s[i - 3][2], s[i - 3][3]); }
            if (i == 5) __builtin_amdgcn_raw_buffer_store_b128(d, rs, l16, xo + qp * (8 * SLAB) + mp * 1024, 16);
            if (i == 6) chk = opq((chk + d[0] + d[1]) + d[2] + d[3]);
            if (i == 7) {
              asm volatile("" :: "v"(d));
              if (mp == 4) { __builtin_amdgcn_raw_buffer_store_b32(chk, rs, l4, xo + qp * (8 * SLAB) + 5120, 16); chk = tag; }
            }
          }
          SB();
        }
      }
      ST(16);
      ST_DUMP();
      __builtin_amdgcn_endpgm();
    }
    const int bp = b >> 1, dq = b & 1;
    unsigned wa[4], xa[4];
#pragma unroll
    for (int i = 0; i < 4; ++i) { wa[i] = wc[i]; xa[i] = opq(lb + (bp * 64 + ((i & 1) ^ dq ^ 1) * 32 + c) * 128 + (((4 * (i >> 1) + g) ^ (c >> 1)) << 4)); }
    unsigned ka[3][2][2];
#pragma unroll
    for (int m = 0; m < 3; ++m)
#pragma unroll
      for (int e = 0; e < 2; ++e) {
        const int M = 3 * dq + m;
        const unsigned a = lb + (M >= 4 ? SLOT : SPARE + (M >> 1) * 16384) + (bp * 64 + (e ^ dq ^ 1) * 32 + c) * 128 +
                           (((4 * (M & 1) + (g >> 1)) ^ (c >> 1)) << 4) + 8 * (g & 1);
        ka[m][e][0] = opq(a); ka[m][e][1] = opq(a ^ 32);
      }
    const unsigned l0 = opq(lane || b ? WS_END : 0u);
    const unsigned swp = opq(lb + 2 * SLOT + b * 4096 + lane * 16), swr = opq(swp ^ 4096);
    f32x4 acc[6][4];
#pragma unroll
    for (int d = 0; d < 6; ++d)
#pragma unroll
      for (int q = 0; q < 4; ++q) { acc[d][q] = (f32x4){}; asm volatile("" : "+v"(acc[d][q])); }
    Frag w[4];
    v4u xf[4];
    warm_more<NBQ>(junk);
    ST(20);
    bar(); ST(1);
    w[0].lo = ldtr(wc[0]); w[0].hi = ldtr(wc[0] + 512);
    SB();
#pragma unroll
    for (int q = 0; q < 4; ++q) { xf[q] = lds128(xa[q >> 1] + (q & 1) * 2048); SB(); }
#pragma unroll
    for (int i = 1; i < 3; ++i) { w[i].lo = ldtr(wc[i]); w[i].hi = ldtr(wc[i] + 512); SB(); }
    unsigned so = 0;
#pragma unroll 1
    for (int n = 0; n < 9; ++n) { const unsigned sn = nxt(so); q_chunk(acc, w, xf, xa, wa, wc, so, sn); so = sn; }
    Frag u[6];
    v4u xn[4], ts;
#pragma unroll
    for (int i = 0; i < 6; ++i) {
      const v4u a = w[i & 3].v();
      acc[i][0] = mfma(a, xf[0], acc[i][0]); SB();
      if (i < 3) w[(i + 3) & 3].lo = ldtr(wq_addr(wa, i + 3)); else xn[i - 3] = lds128(xa[2 + ((i - 3) >> 1)] + ((i - 3) & 1) * 2048);
      SB();
      acc[i][1] = mfma(a, xf[1], acc[i][1]); SB();
      if (i < 3) w[(i + 3) & 3].hi = ldtr(wq_addr(wa, i + 3) + 512);
      if (i == 5) xn[3] = lds128(xa[3] + 2048);
      if (i == 3) tag = *tagw;
      if (i == 4) ts = lds128(tsa + lane * 16);
      SB();
      acc[i][2] = mfma(a, xf[2], acc[i][2]); SB();
      u[i].lo = ldtr(wq_addr(wa, i + 6)); SB();
      acc[i][3] = mfma(a, xf[3], acc[i][3]); SB();
      u[i].hi = ldtr(wq_addr(wa, i + 6) + 512); SB();
    }
    v4u qf[2][3][2];
    Frag kf[2][3][2];
    f32x4 s[2][2][2], sp[2][2];
    auto kread = [&](int e, int m, int r) {
      if (r & 1) kf[e][m][r >> 1].hi = lds64(ka[m][e][1] + (r >> 1) * 2048); else kf[e][m][r >> 1].lo = lds64(ka[m][e][0] + (r >> 1) * 2048);
      SB();
    };
#pragma unroll
    for (int i = 0; i < 48; ++i) {
      if (i == 12) { bar(); ST(13); }
      if (i == 42) bar();
      SB();
      if (i < 24) {
        const int dt = (i % 12) >> 1, q = (i / 12) * 2 + (i & 1);
        acc[dt][q] = mfma(u[dt].v(), xn[q], acc[dt][q]);
      } else {
        const int e = (i - 24) / 12, T = (i % 12) / 3, m = i % 3;
        s[e][T >> 1][T & 1] = mfma(kf[e][m][T & 1].v(), qf[e][m][T >> 1], m ? s[e][T >> 1][T & 1] : (f32x4){});
      }
      SB();
      if (i >= 12 && i < 24) kread(0, ((i - 12) >> 1) % 3, ((i - 12) / 6) * 2 + (i & 1));
      if (i >= 24 && i < 36) kread(1, ((i - 24) >> 1) % 3, ((i - 24) / 6) * 2 + (i & 1));
      if (i >= 4 && i < 28) {
        const int n = i - 4, e = n / 12, dt = (n % 12) >> 1, q = n & 1;
        const f32x4& a = acc[dt][2 * e + q];
        qf[e][dt >> 1][q][2 * (dt & 1)] = pk(a[0], a[1]); qf[e][dt >> 1][q][2 * (dt & 1) + 1] = pk(a[2], a[3]);
      }
      if (i >= 29 && i < 39 && (i - 29) % 3 == 0) { const int T = (i - 29) / 3; *(LAS f32x4*)(swp + T * 1024) = s[0][T >> 1][T & 1]; }
      if (i >= 42 && i < 46) sp[(i - 42) >> 1][i & 1] = *(const LAS f32x4*)(swr + (i - 42) * 1024);
      SB();
    }
    tag = __builtin_amdgcn_readfirstlane(tag);
    ST(14);
    {
      auto announce = [&](int at) { if (at == HQ) { SB(); __builtin_amdgcn_raw_buffer_store_b32(tag, rs, l0, WS_H + bid * 128, 16); SB(); } };
      announce(0);
      const float cs = 0.07216878364870323f * 1.4426950408889634f;
      float m[2], l[2];
#pragma unroll
      for (int q = 0; q < 2; ++q) {
        sp[q][0] += s[1][q][0]; sp[q][1] += s[1][q][1];
        m[q] = vmax3(vmax3(sp[q][0][0], sp[q][0][1], sp[q][0][2]), vmax3(sp[q][0][3], sp[q][1][0], sp[q][1][1]), vmax(sp[q][1][2], sp[q][1][3]));
      }
      xred<true>(m[0], m[1]);
      announce(1);
#pragma unroll
      for (int q = 0; q < 2; ++q) {
#pragma unroll
        for (int r = 0; r < 8; ++r) sp[q][r >> 2][r & 3] = __builtin_amdgcn_exp2f(fmaf(sp[q][r >> 2][r & 3], cs, m[q]));
        const f32x4 t = sp[q][0] + sp[q][1];
        l[q] = (t[0] + t[1]) + (t[2] + t[3]);
      }
      announce(2);
      xred<false>(l[0], l[1]);
      announce(3);
#pragma unroll
      for (int q = 0; q < 2; ++q) {
        const v4u pv = (pack2(sp[q][0] * l[q], sp[q][1] * l[q]) & 0x7fff7fffu) | ts;
        __builtin_amdgcn_raw_buffer_store_b128(pv, rs, lane * 16 + q * 1024, WS_P + (((xi * 4 + rg) * 4 + j) * 4 + b) * 2048, 16);
        asm volatile("s_nop 1" :: "v"(pv) : "memory");
        SB();
      }
      announce(4);
    }
    ST(15);
  }
  const unsigned c = lane & 15, g = lane >> 4;
  v4u id0, id1;
  { const unsigned one = (c & 1) ? 0x3f800000u : 0x00003f80u, j2 = c >> 1;
    id0 = (v4u){j2 == 0 ? one : 0u, j2 == 1 ? one : 0u, j2 == 2 ? one : 0u, j2 == 3 ? one : 0u};
    id1 = (v4u){j2 == 4 ? one : 0u, j2 == 5 ? one : 0u, j2 == 6 ? one : 0u, j2 == 7 ? one : 0u}; }
  const int late = wv >> 2, dst = (xi & 6) | late, un = b * 2 + (xi & 1);
  const unsigned o = WS_X + (((rg * 4 + j) * 8 + dst) * 8 + (lane >> 3)) * SLAB;
  unsigned vo = o + (un * 8 + (lane & 7)) * 16, vo4 = vo + 4096, vk = o + 5120 + (un * 8 + (lane & 7)) * 4;
  unsigned oo = (rg * 128 + dst * 16 + (xi & 1) * 8 + (c & 7)) * 1280 + (j * 160 + (c >> 3) * 64 + g * 16 + b * 4) * 2;
  asm volatile("" : "+v"(id0), "+v"(id1), "+v"(vo), "+v"(vo4), "+v"(vk), "+v"(oo));
  poll(hint + (128 + (rg * 32 + j * 8 + wv) * 8 + (xi & 6)) * 32, tag);
  nap<DR>();
  bar();
  if (late) { asm volatile("s_barrier" ::: "memory"); nap<DL>(); }
  ST(17);
  v4u d[5];
  unsigned want;
  f32x4 s0, s1, s2;
  auto fetch = [&]() {
#pragma unroll
    for (int m = 0; m < 5; ++m) d[m] = __builtin_amdgcn_raw_buffer_load_b128(rs, m < 4 ? vo + m * 1024 : vo4, 0, 16);
    want = __builtin_amdgcn_raw_buffer_load_b32(rs, vk, 0, 16);
  };
  auto stale = [&]() {
    unsigned chk = tag;
#pragma unroll
    for (int m = 0; m < 5; ++m) {
      f32x4& s = m < 2 ? s0 : m < 4 ? s1 : s2;
      s = mfma(m & 1 ? id1 : id0, d[m], m & 1 ? s : (f32x4){});
      chk = (chk + d[m][0] + d[m][1]) + d[m][2] + d[m][3];
      SB();
    }
    return !__all(chk == want);
  };
  fetch();
  if (!late) { wait_vm<LW>(); asm volatile("s_barrier" ::: "memory"); }
  if (__builtin_expect(stale(), 0)) {
#pragma unroll 1
    for (int spins = 0; spins < kSpinCap; ++spins) {
      ST(19);
      nap<4>();
      fetch();
      if (!stale()) break;
    }
  }
#pragma unroll
  for (int r = 0; r < 4; ++r) {
    const float keep = c < 8 ? s0[r] : s1[r], send = c < 8 ? s1[r] : s0[r];
    s0[r] = keep + __uint_as_float(__builtin_amdgcn_update_dpp(0u, __float_as_uint(send), 0x128, 0xf, 0xf, true));
    s2[r] += __uint_as_float(__builtin_amdgcn_update_dpp(0u, __float_as_uint(s2[r]), 0x128, 0xf, 0xf, true));
  }
  *(GAS v2u*)(out + oo) = (v2u){pk(s0[0], s0[1]), pk(s0[2], s0[3])};
  if (c < 8 && g < 2) *(GAS v2u*)(out + oo + 256) = (v2u){pk(s2[0], s2[1]), pk(s2[2], s2[3])};
  ST(18);
  ST_DUMP();
#undef ws
#undef out
#undef rs
#undef hint
}

extern "C" void kernel_launch(void* const* d_in, const int* in_sizes, int n_in, void* d_out, int out_size,
                              void* d_ws, size_t ws_size, hipStream_t stream) {
  mha_block_kernel<<<256, 512, 0, stream>>>((const char*)d_in[0], (const char*)d_in[1], (const char*)d_in[2],
                                            (const char*)d_in[3], (const char*)d_in[4], (char*)d_out, (char*)d_ws);
}
```
